```python
import torch

def _softmax(x, axis=-1):
    return torch.softmax(x, dim=axis)
def _gelu(x):
    return torch.nn.functional.gelu(x, approximate="tanh")
def _silu(x):
    return torch.nn.functional.silu(x)
def _sigmoid(x):
    return torch.sigmoid(x)
def _layernorm(x, axis=-1, eps=1e-5):
    m = x.mean(dim=axis, keepdim=True)
    v = x.var(dim=axis, keepdim=True, unbiased=False)
    return (x - m) / torch.sqrt(v + eps)
def _scatter_add(tgt, idx, src, axis=0):
    return tgt.clone().index_add_(axis, idx, src)
def _segment_sum(src, seg, n):
    out = torch.zeros((n,) + src.shape[1:], dtype=src.dtype)
    return out.index_add_(0, seg, src)
def _pad(x, pw):
    flat = tuple(v for pair in reversed(pw) for v in pair)
    return torch.nn.functional.pad(x, flat)

kernel_name = 'interleave_layers__sliding_block_cached__dense_activation_silu__decode'

def setup_inputs():

    _w = (lambda a: (lambda t: t.to(torch.bfloat16) if t.is_floating_point() else t)(torch.as_tensor(a)))
    def _A_setup():
        B, Sq, D = 4, 1, 512
        Hq, Hkv, Sk, Dh = 32, 8, 4096, 128
        assert Hq % Hkv == 0, (Hq, Hkv)
        return {
            "x": _w((lambda *s: torch.randn(*s, dtype=torch.bfloat16) * 0.1)(B, Sq, D)),
            "k_cache": _w((lambda *s: torch.randn(*s, dtype=torch.bfloat16) * 0.8)(B, Hkv, Sk, Dh)),
            "v_cache": _w((lambda *s: torch.randn(*s, dtype=torch.bfloat16) * 3.2)(B, Hkv, Sk, Dh)),
            "wq": _w((lambda *s: torch.randn(*s, dtype=torch.bfloat16) * 0.8)(D, Hq * Dh) * (D ** -0.5)),
            "wo": _w((lambda *s: torch.randn(*s, dtype=torch.bfloat16) * 3.2)(Hq * Dh, D) * ((Hq * Dh) ** -0.5)),
        }
    _A_in = _A_setup()

    def _B_setup():
        B, S, D, F = 4, 1, 512, 1792
        return {
            "x": _w((lambda *s: torch.randn(*s, dtype=torch.bfloat16) * 0.1)(B, S, D)),
            "w1": _w((lambda *s: torch.randn(*s, dtype=torch.bfloat16) * 1.6)(D, F) * (D ** -0.5)),
            "w2": _w((lambda *s: torch.randn(*s, dtype=torch.bfloat16) * 0.4)(F, D) * (F ** -0.5)),
        }
    _B_in = _B_setup()

    return {
        "A_x": _A_in["x"],
        "A_k_cache": _A_in["k_cache"],
        "A_v_cache": _A_in["v_cache"],
        "A_wq": _A_in["wq"],
        "A_wo": _A_in["wo"],
        "B_x": _B_in["x"],
        "B_w1": _B_in["w1"],
        "B_w2": _B_in["w2"],
    }

def reference(A_x, A_k_cache, A_v_cache, A_wq, A_wo, B_x, B_w1, B_w2):
    def _A_ref(x, k_cache, v_cache, wq, wo):
        B, Sq, D = x.shape
        Hq, Dh = 32, 128
        Hkv, Sk = k_cache.shape[1], k_cache.shape[2]
        W = min(4096, max(256, Sk // 4))
        rep = Hq // Hkv
        q = (lambda x, *d: x.permute(*d))((x @ wq).reshape(B, Sq, Hq, Dh), 0, 2, 1, 3)
        k = (lambda x, n, axis: torch.repeat_interleave(x, n, dim=axis))(k_cache[:, :, Sk - W:, :], rep, axis=1)
        v = (lambda x, n, axis: torch.repeat_interleave(x, n, dim=axis))(v_cache[:, :, Sk - W:, :], rep, axis=1)
        scores = torch.einsum("bhqd,bhkd->bhqk", q, k) * (Dh ** -0.5)
        p = _softmax(scores, axis=-1)
        o = torch.einsum("bhqk,bhkd->bhqd", p, v)
        return (lambda x, *d: x.permute(*d))(o, 0, 2, 1, 3).reshape(B, Sq, Hq * Dh) @ wo

    def _B_ref(x, w1, w2):
        h = torch.einsum("bsd,df->bsf", x, w1)
        h = _silu(h)
        out = torch.einsum("bsf,fd->bsd", h, w2)
        return out

    x = A_x
    x = x + _A_ref(_layernorm(x, axis=-1), A_k_cache, A_v_cache, A_wq, A_wo)
    x = x + _B_ref(_layernorm(x, axis=-1), B_w1, B_w2)
    x = x + _A_ref(_layernorm(x, axis=-1), A_k_cache, A_v_cache, A_wq, A_wo)
    x = x + _B_ref(_layernorm(x, axis=-1), B_w1, B_w2)
    return x
```

```cpp
#include <hip/hip_runtime.h>
#include <hip/hip_bf16.h>
#include <stdint.h>

typedef unsigned short u16;
__device__ __forceinline__ float bf2f(u16 b) { return __uint_as_float(((unsigned)b) << 16); }
__device__ __forceinline__ u16 f2bf(float f) { return __builtin_bit_cast(u16, (__bf16)f); }
__device__ __forceinline__ float rbf(float f) { return bf2f(f2bf(f)); }

constexpr int B = 4, D = 512, HQ = 32, HKV = 8, SK = 4096, DH = 128, W = 1024, F = 1792;

__device__ void ln_rows(const float* x, float* xn  , float* red) {
  int tid = threadIdx.x;
  int w = tid >> 6, lane = tid & 63;
  float v[8]; float s = 0.f;
  for (int i = 0; i < 8; ++i) { v[i] = x[w * D + lane + 64 * i]; s += v[i]; }
  for (int m = 32; m >= 1; m >>= 1) s += __shfl_xor(s, m);
  float mean = s / D;
  float q = 0.f;
  for (int i = 0; i < 8; ++i) { float d = v[i] - mean; q += d * d; }
  for (int m = 32; m >= 1; m >>= 1) q += __shfl_xor(q, m);
  float var = rbf(q / D);
  float mb = rbf(mean);
  float den = rbf(sqrtf(rbf(var + 1e-5f)));
  for (int i = 0; i < 8; ++i) xn[w * D + lane + 64 * i] = rbf(rbf(v[i] - mb) / den);
}

__global__ void k_init(const u16* x, float* xf) { int i = blockIdx.x * 256 + threadIdx.x; if (i < B * D) xf[i] = bf2f(x[i]); }

template <int N, int ACT>
__global__ void k_ln_gemm(const float* x, const u16* w, float* out) {
  __shared__ float xn[B * D]; __shared__ float red[16][16][4];
  ln_rows(x, xn, nullptr);
  __syncthreads();
  int c = threadIdx.x & 15, g = threadIdx.x >> 4;
  int n = blockIdx.x * 16 + c;
  float a[4] = {0, 0, 0, 0};
  for (int k = g; k < D; k += 16) { float wv = bf2f(w[(size_t)k * N + n]); for (int b = 0; b < 4; ++b) a[b] += xn[b * D + k] * wv; }
  for (int b = 0; b < 4; ++b) red[g][c][b] = a[b];
  __syncthreads();
  if (threadIdx.x < 64) { int cc = threadIdx.x & 15, b = threadIdx.x >> 4; float s = 0; for (int gg = 0; gg < 16; ++gg) s += red[gg][cc][b];
    s = rbf(s); if (ACT) s = rbf(s / (1.f + __expf(-s))); out[b * N + blockIdx.x * 16 + cc] = s; }
}
__global__ void k_attn(const float* q, const u16* kc, const u16* vc, float* o) {
  __shared__ float qs[DH]; __shared__ float sc[W]; __shared__ float red[256];
  int b = blockIdx.x / HQ, h = blockIdx.x % HQ, hk = h / 4, tid = threadIdx.x;
  if (tid < DH) qs[tid] = q[b * HQ * DH + h * DH + tid];
  __syncthreads();
  const u16* kb = kc + ((size_t)(b * HKV + hk) * SK + (SK - W)) * DH;
  const u16* vb = vc + ((size_t)(b * HKV + hk) * SK + (SK - W)) * DH;
  float mx = -INFINITY;
  for (int j = tid; j < W; j += 256) { float s = 0; for (int d = 0; d < DH; ++d) s += qs[d] * bf2f(kb[(size_t)j * DH + d]); s = rbf(rbf(s) * 0.08838834764831845f); sc[j] = s; mx = fmaxf(mx, s); }
  red[tid] = mx; __syncthreads();
  for (int s = 128; s > 0; s >>= 1) { if (tid < s) red[tid] = fmaxf(red[tid], red[tid + s]); __syncthreads(); }
  mx = red[0]; __syncthreads();
  float sum = 0; for (int j = tid; j < W; j += 256) { float e = __expf(sc[j] - mx); sc[j] = e; sum += e; }
  red[tid] = sum; __syncthreads();
  for (int s = 128; s > 0; s >>= 1) { if (tid < s) red[tid] += red[tid + s]; __syncthreads(); }
  sum = red[0]; __syncthreads();
  int d = tid & 127, half = tid >> 7; float a = 0;
  for (int j = half; j < W; j += 2) a += rbf(sc[j] / sum) * bf2f(vb[(size_t)j * DH + d]);
  red[tid] = a; __syncthreads();
  if (tid < 128) o[b * HQ * DH + h * DH + tid] = rbf(red[tid] + red[tid + 128]);
}
template <int K>
__global__ void k_gemm_res(const float* in, const u16* w, float* x, u16* out) {
  __shared__ float red[16][16][4];
  int c = threadIdx.x & 15, g = threadIdx.x >> 4;
  int n = blockIdx.x * 16 + c;
  float a[4] = {0, 0, 0, 0};
  for (int k = g; k < K; k += 16) { float wv = bf2f(w[(size_t)k * D + n]); for (int b = 0; b < 4; ++b) a[b] += in[b * K + k] * wv; }
  for (int b = 0; b < 4; ++b) red[g][c][b] = a[b];
  __syncthreads();
  if (threadIdx.x < 64) { int cc = threadIdx.x & 15, b = threadIdx.x >> 4; float s = 0; for (int gg = 0; gg < 16; ++gg) s += red[gg][cc][b];
    int idx = b * D + blockIdx.x * 16 + cc; float r = rbf(x[idx] + rbf(s)); x[idx] = r; if (out) out[idx] = f2bf(r); }
}

extern "C" void kernel_launch(void* const* d_in, const int* in_sizes, int n_in, void* d_out, int out_size, void* d_ws, size_t ws_size, hipStream_t stream) {
  const u16* x = (const u16*)d_in[0]; const u16* kc = (const u16*)d_in[1]; const u16* vc = (const u16*)d_in[2];
  const u16* wq = (const u16*)d_in[3]; const u16* wo = (const u16*)d_in[4]; const u16* w1 = (const u16*)d_in[6]; const u16* w2 = (const u16*)d_in[7];
  float* ws = (float*)d_ws; float* xf = ws; float* q = ws + 4096; float* o = q + B * 4096; float* h = o + B * 4096;
  k_init<<<8, 256, 0, stream>>>(x, xf);
  for (int l = 0; l < 2; ++l) {
    k_ln_gemm<4096, 0><<<256, 256, 0, stream>>>(xf, wq, q);
    k_attn<<<B * HQ, 256, 0, stream>>>(q, kc, vc, o);
    k_gemm_res<4096><<<32, 256, 0, stream>>>(o, wo, xf, nullptr);
    k_ln_gemm<F, 1><<<F / 16, 256, 0, stream>>>(xf, w1, h);
    k_gemm_res<F><<<32, 256, 0, stream>>>(h, w2, xf, l == 1 ? (u16*)d_out : nullptr);
  }
}
```

```cpp
#include <hip/hip_runtime.h>
#include <hip/hip_bf16.h>

typedef unsigned u32;
typedef unsigned long long u64;
typedef unsigned short u16;
typedef u32 v4u __attribute__((ext_vector_type(4)));
typedef u32 v2u __attribute__((ext_vector_type(2)));
typedef float f4 __attribute__((ext_vector_type(4)));
typedef float f2 __attribute__((ext_vector_type(2)));
typedef __bf16 bf8 __attribute__((ext_vector_type(8)));
typedef short s4 __attribute__((ext_vector_type(4)));
#define LAS __attribute__((address_space(3)))
#define GAS __attribute__((address_space(1)))
#define DEVI __device__ __forceinline__

#define STAMPS 0
#define STAMP_WAVE 0
#define DLY_A 22
#define DLY_B 22
#define DLY_C 22
#define DLY_D 22
#define DLY_E 22
#define NAPN 1

constexpr int DM = 512, SK = 4096, WIN = 1024, DH = 128, FF = 1792, NQ = 4096;
constexpr int WS_EPOCH = 0;
constexpr int WS_FLAGS = 4096;
constexpr int WS_QG = 8192;
constexpr int WS_SCRUB = WS_QG + 65536;
constexpr int OB_L = 262144;
constexpr int WS_OB = WS_SCRUB;
constexpr int ML_L = 32768;
constexpr int WS_ML = WS_OB + 2 * OB_L;
constexpr int CB_L = 32768;
constexpr int WS_CB = WS_ML + 2 * ML_L;
constexpr int DB_L = 4 * 8 * 56 * 128;
constexpr int WS_DB = WS_CB + 2 * CB_L;
constexpr int WS_EB = WS_DB + 2 * DB_L;
constexpr int WS_QD = WS_EB + 4096;
constexpr int SCRUB_PER = 4608;
constexpr int SCRUB_BYTES = 256 * SCRUB_PER;
static_assert(WS_QD + 32768 <= WS_SCRUB + SCRUB_BYTES, "scrub covers every sentinel slot");
constexpr int WS_END = WS_SCRUB + SCRUB_BYTES;
constexpr u32 SENT = 0xFFFFFFFFu;
constexpr u32 SPIN_CAP = 1u << 17;
constexpr int AUX_SC1 = 16;

constexpr int L_W1 = 0;
constexpr int L_RED = 32768;
constexpr int L_QCS = L_RED + 1024;
constexpr int L_ST = L_QCS + 1024;
constexpr int L_AO = L_ST + 64;
constexpr int L_AML = L_AO + 8192;
constexpr int L_X1 = L_AML + 128;
constexpr int L_H = L_X1 + 1024;
constexpr int L_XS = L_H + 64;
constexpr int L_END = L_XS + 1024;
constexpr int LDS_BYTES = 84 * 1024;
static_assert(L_END <= LDS_BYTES, "lds");

DEVI float asf(u32 x) { return __builtin_bit_cast(float, x); }
DEVI u32 asu(float x) { return __builtin_bit_cast(u32, x); }
DEVI float bf_lo(u32 x) { return asf(x << 16); }
DEVI float bf_hi(u32 x) { return asf(x & 0xffff0000u); }
DEVI u32 pk_bf16(float lo, float hi) {
  typedef __bf16 b2 __attribute__((ext_vector_type(2)));
  b2 r = {(__bf16)lo, (__bf16)hi};
  return __builtin_bit_cast(u32, r);
}
DEVI u32 nosent(u32 x) { return x > 0xFFFFFFFEu ? 0xFFFFFFFEu : x; }
DEVI float rbf(float f) { return bf_lo(pk_bf16(f, 0.f)); }
DEVI float vmax(float a, float b) { float r; asm("v_max_f32 %0, %1, %2" : "=v"(r) : "v"(a), "v"(b)); return r; }
DEVI float vmax3(float a, float b, float c) { float r; asm("v_max3_f32 %0, %1, %2, %3" : "=v"(r) : "v"(a), "v"(b), "v"(c)); return r; }
DEVI f4 mfma(v4u a, v4u b, f4 c) { return __builtin_amdgcn_mfma_f32_16x16x32_bf16(__builtin_bit_cast(bf8, a), __builtin_bit_cast(bf8, b), c, 0, 0, 0); }
template <int CTRL> DEVI float dppf(float x) { return asf((u32)__builtin_amdgcn_mov_dpp((int)asu(x), CTRL, 0xf, 0xf, true)); }
template <int CTRL> DEVI u32 dppu(u32 x) { return (u32)__builtin_amdgcn_mov_dpp((int)x, CTRL, 0xf, 0xf, true); }
constexpr int XOR1 = 0xB1, XOR2 = 0x4E, ROR2 = 0x122, ROR4 = 0x124, ROR8 = 0x128, ROR12 = 0x12C;
DEVI float tsum32(float a, float b) { auto s = __builtin_amdgcn_permlane32_swap(asu(a), asu(b), false, false); return asf(s[0]) + asf(s[1]); }
DEVI float tsum16(float a, float b) { auto s = __builtin_amdgcn_permlane16_swap(asu(a), asu(b), false, false); return asf(s[0]) + asf(s[1]); }
DEVI float amax16(float a) { auto s = __builtin_amdgcn_permlane16_swap(asu(a), asu(a), false, false); return vmax(asf(s[0]), asf(s[1])); }
DEVI float amax32(float a) { auto s = __builtin_amdgcn_permlane32_swap(asu(a), asu(a), false, false); return vmax(asf(s[0]), asf(s[1])); }
DEVI float asum16(float a) { return tsum16(a, a); }
DEVI bool ok4(v4u x) { return (x.x != SENT) & (x.y != SENT) & (x.z != SENT) & (x.w != SENT); }
DEVI bool ok2(v2u x) { return (x.x != SENT) & (x.y != SENT); }
DEVI v4u ld_sc1(__amdgpu_buffer_rsrc_t rs, int off) { return __builtin_amdgcn_raw_buffer_load_b128(rs, off, 0, AUX_SC1); }
DEVI v2u ld_sc1_64(__amdgpu_buffer_rsrc_t rs, int off) { return __builtin_amdgcn_raw_buffer_load_b64(rs, off, 0, AUX_SC1); }
DEVI void st_sc1(__amdgpu_buffer_rsrc_t rs, int off, v4u v) { __builtin_amdgcn_raw_buffer_store_b128(v, rs, off, 0, AUX_SC1); }
DEVI void st_sc1_32(__amdgpu_buffer_rsrc_t rs, int off, u32 v) { __builtin_amdgcn_raw_buffer_store_b32(v, rs, off, 0, AUX_SC1); }
DEVI void st_sc1_64(__amdgpu_buffer_rsrc_t rs, int off, v2u v) { __builtin_amdgcn_raw_buffer_store_b64(v, rs, off, 0, AUX_SC1); }
template <int N> DEVI void delay() {
#pragma unroll
  for (int i = 0; i < N; ++i) asm volatile("s_nop 5" ::: "memory");
}
#define SB() __builtin_amdgcn_sched_barrier(0)
#define LOOPFENCE() asm volatile("" ::: "memory")
#define NAP() __builtin_amdgcn_s_sleep(NAPN)
DEVI void bar() { asm volatile("s_waitcnt lgkmcnt(0)" ::: "memory"); __builtin_amdgcn_s_barrier(); asm volatile("" ::: "memory"); }
DEVI void transpose8(const v4u (&R)[8], v4u (&F)[8]) {
#pragma unroll
  for (int t = 0; t < 8; ++t)
#pragma unroll
    for (int m = 0; m < 4; ++m) F[t][m] = __builtin_amdgcn_perm(R[2 * m + 1][t >> 1], R[2 * m][t >> 1], (t & 1) ? 0x07060302u : 0x05040100u);
}
DEVI void transpose84(const v2u (&R)[8], v4u* F) {
#pragma unroll
  for (int t = 0; t < 4; ++t)
#pragma unroll
    for (int m = 0; m < 4; ++m) F[t][m] = __builtin_amdgcn_perm(R[2 * m + 1][t >> 1], R[2 * m][t >> 1], (t & 1) ? 0x07060302u : 0x05040100u);
}
DEVI v4u tr_frag(LAS unsigned char* p0, LAS unsigned char* p1) {
  s4 a = __builtin_amdgcn_ds_read_tr16_b64_v4i16((LAS s4*)p0), b = __builtin_amdgcn_ds_read_tr16_b64_v4i16((LAS s4*)p1);
  v2u ua = __builtin_bit_cast(v2u, a), ub = __builtin_bit_cast(v2u, b);
  return v4u{ua.x, ua.y, ub.x, ub.y};
}
#define STAMP(i) do { } while (0)
#define FINE(i) do { } while (0)
#define POLL(OK, RELOAD) do { if (!__all(OK)) { for (u32 sp_ = 0; sp_ < SPIN_CAP; ++sp_) { LOOPFENCE(); NAP(); RELOAD; if (__all(OK)) break; } } } while (0)

struct Params { const u16* x; const u16* kc; const u16* vc; const u16* wq; const u16* wo; const u16* w1; const u16* w2; u16* out; unsigned char* ws; };

struct Ctx {
  int tid, lane, w, j, g;
  int hkv, b, s, cc, bh;
  u32 tag; u64 flagv;
  __amdgpu_buffer_rsrc_t ws;
  LAS unsigned char* L;
};

template <int LAYER>
DEVI void layer(const Params& p, const Ctx& c, v4u (&Wq)[16], v4u (&Kf)[8], v4u (&Vf)[8], v4u (&Wo)[16], v4u (&W1)[8], v4u (&W2)[8],
                float& csq, float& cs1, v4u& xa, u32 (&stamps)[32]) {
  const int tid = c.tid, lane = c.lane, w = c.w, j = c.j, g = c.g;
  LAS unsigned char* L = c.L;
  if (LAYER == 1) {
    delay<DLY_E>();
    const int eoff = WS_EB + c.b * 1024 + (128 * w + 32 * (j & 3) + 8 * g) * 2;
    xa = ld_sc1(c.ws, eoff);
    POLL(ok4(xa), if (!ok4(xa)) xa = ld_sc1(c.ws, eoff));
    STAMP(12);
  }
  {
    f4 acc[4];
#pragma unroll
    for (int st = 0; st < 4; ++st) {
      v4u A;
#pragma unroll
      for (int e = 0; e < 4; ++e) A[e] = st == 0 ? dppu<0x00>(xa[e]) : st == 1 ? dppu<0x55>(xa[e]) : st == 2 ? dppu<0xAA>(xa[e]) : dppu<0xFF>(xa[e]);
#pragma unroll
      for (int t = 0; t < 4; ++t) acc[t] = mfma(A, Wq[st * 4 + t], st == 0 ? f4{0.f, 0.f, 0.f, 0.f} : acc[t]);
    }
    float s1 = 0.f, s2 = 0.f;
#pragma unroll
    for (int e = 0; e < 4; ++e) { float a = bf_lo(xa[e]), d = bf_hi(xa[e]); s1 += a + d; s2 = fmaf(a, a, fmaf(d, d, s2)); }
    s1 += dppf<XOR1>(s1); s2 += dppf<XOR1>(s2); s1 += dppf<XOR2>(s1); s2 += dppf<XOR2>(s2);
    { const float t = tsum32(s1, s2); s1 = asum16(t); }
    if ((lane & 31) == 0) *(LAS float*)(L + L_ST + w * 8 + (lane >> 5) * 4) = s1;
    if (j < 4) *(LAS v4u*)(L + L_XS + (128 * w + 32 * j + 8 * g) * 2) = xa;
    if (g == 0) *(LAS f4*)(L + L_RED + (w * 64 + 4 * j) * 4) = f4{acc[0][0], acc[1][0], acc[2][0], acc[3][0]};
    if (LAYER == 0) {
      const v4u ones = {0x3F803F80u, 0x3F803F80u, 0x3F803F80u, 0x3F803F80u};
      f4 a1[4];
#pragma unroll
      for (int t = 0; t < 4; ++t) { a1[t] = f4{0.f, 0.f, 0.f, 0.f};
#pragma unroll
        for (int st = 0; st < 4; ++st) a1[t] = mfma(ones, Wq[st * 4 + t], a1[t]); }
      if (g == 0) *(LAS f4*)(L + L_QCS + (w * 64 + 4 * j) * 4) = f4{a1[0][0], a1[1][0], a1[2][0], a1[3][0]};
    }
    bar();
    if (w == 0) {
      float d = 0.f; f2 st = {0.f, 0.f};
#pragma unroll
      for (int ww = 0; ww < 4; ++ww) { d += *(LAS float*)(L + L_RED + (ww * 64 + lane) * 4); st += *(LAS f2*)(L + L_ST + ww * 8); }
      if (LAYER == 0) { csq = 0.f;
#pragma unroll
        for (int ww = 0; ww < 4; ++ww) csq += *(LAS float*)(L + L_QCS + (ww * 64 + lane) * 4); }
      const float mean = st.x * (1.f / DM), var = fmaxf(st.y * (1.f / DM) - mean * mean, 0.f), rstd = __builtin_amdgcn_rsqf(var + 1e-5f);
      const float q = rstd * (d - mean * csq);
      const u32 pk = pk_bf16(q, dppf<XOR1>(q));
      if (!(lane & 1)) {
        if (LAYER == 0) st_sc1_64(c.ws, WS_QG + (c.bh * 8 + c.s) * 256 + ((lane >> 2) & 1) * 128 + (lane >> 3) * 16 + ((lane >> 1) & 1) * 8, v2u{pk, c.tag});
        else st_sc1_32(c.ws, WS_QD + (c.bh * 512 + 64 * c.s + lane) * 2, nosent(pk));
      }
      if (LAYER == 0 && lane == 0) st_sc1_64(c.ws, WS_FLAGS + blockIdx.x * 8, v2u{(u32)c.flagv, (u32)(c.flagv >> 32)});
    }
  }
  STAMP(LAYER * 12 + 1);
  v4u Vraw[8]; v2u Woraw[4][8];
  v4u qf[4];
  {
    v4u X;
    const int ch = 4 * (j >> 2) + g;
    FINE(0);
    if (LAYER == 0) {
      const int qoff = WS_QG + (c.bh * 8 + 2 * (j & 3) + (ch >> 3)) * 256 + (ch & 7) * 16;
      v4u qa = ld_sc1(c.ws, qoff), qb = ld_sc1(c.ws, qoff + 128);
      v4u fl0 = ld_sc1(c.ws, WS_FLAGS + lane * 32), fl1 = ld_sc1(c.ws, WS_FLAGS + lane * 32 + 16);
      SB();
      const GAS unsigned char* vb = (const GAS unsigned char*)(p.vc + ((size_t)c.bh * SK + (SK - WIN) + c.s * 128 + 32 * w) * DH);
#pragma unroll
      for (int rr = 0; rr < 8; ++rr) Vraw[rr] = *(const GAS v4u*)(vb + ((rr < 4 ? 4 * g + rr : 16 + 4 * g + rr - 4) * DH + 8 * j) * 2);
      const GAS unsigned char* ob = (const GAS unsigned char*)(p.wo + (size_t)(c.hkv * 512 + 128 * w + 8 * g) * DM + 64 * c.s + 4 * j);
#pragma unroll
      for (int st = 0; st < 4; ++st)
#pragma unroll
        for (int rr = 0; rr < 8; ++rr) Woraw[st][rr] = *(const GAS v2u*)(ob + (32 * st + rr) * DM * 2);
      SB();
#define QOK ((qa.y == c.tag) & (qa.w == c.tag) & (qb.y == c.tag) & (qb.w == c.tag))
      POLL(QOK, if (!QOK) { qa = ld_sc1(c.ws, qoff); qb = ld_sc1(c.ws, qoff + 128); });
      X = v4u{qa.x, qa.z, qb.x, qb.z};
      const u32 lo = (u32)c.flagv, hi = (u32)(c.flagv >> 32);
#define FOK ((fl0.x == lo) & (fl0.y == hi) & (fl0.z == lo) & (fl0.w == hi) & (fl1.x == lo) & (fl1.y == hi) & (fl1.z == lo) & (fl1.w == hi))
      POLL(FOK, if (!FOK) { fl0 = ld_sc1(c.ws, WS_FLAGS + lane * 32); fl1 = ld_sc1(c.ws, WS_FLAGS + lane * 32 + 16); });
      if (blockIdx.x == 0 && tid == 0) st_sc1_64(c.ws, WS_EPOCH, v2u{lo, hi});
    } else {
      const int qoff = WS_QD + c.bh * 1024 + (j & 3) * 256 + ch * 16;
      delay<DLY_A>();
      FINE(1);
      X = ld_sc1(c.ws, qoff);
      POLL(ok4(X), if (!ok4(X)) X = ld_sc1(c.ws, qoff));
      FINE(2);
    }
    qf[0] = X;
#pragma unroll
    for (int e = 0; e < 4; ++e) { qf[1][e] = dppu<ROR12>(X[e]); qf[2][e] = dppu<ROR8>(X[e]); qf[3][e] = dppu<ROR4>(X[e]); }
  }
  STAMP(LAYER * 12 + 2);
  {
    constexpr float SC = 0.08838834764831845f * 1.4426950408889634f;
    f4 s0 = {0.f, 0.f, 0.f, 0.f}, s1 = {0.f, 0.f, 0.f, 0.f};
#pragma unroll
    for (int st = 0; st < 4; ++st) { s0 = mfma(Kf[st], qf[st], s0); s1 = mfma(Kf[4 + st], qf[st], s1); }
    float m = vmax(vmax3(s0[0], s0[1], s0[2]), vmax3(s0[3], s1[0], s1[1]));
    m = vmax3(m, s1[2], s1[3]);
    m = amax32(amax16(m));
    const float ms = m * SC;
    float pr[8];
#pragma unroll
    for (int e = 0; e < 4; ++e) { pr[e] = __builtin_amdgcn_exp2f(fmaf(s0[e], SC, -ms)); pr[4 + e] = __builtin_amdgcn_exp2f(fmaf(s1[e], SC, -ms)); }
    const v4u P = {pk_bf16(pr[0], pr[1]), pk_bf16(pr[2], pr[3]), pk_bf16(pr[4], pr[5]), pk_bf16(pr[6], pr[7])};
    if (LAYER == 0) transpose8(Vraw, Vf);
    f4 o[8];
#pragma unroll
    for (int t = 0; t < 8; ++t) o[t] = mfma(P, Vf[t], f4{0.f, 0.f, 0.f, 0.f});
    float l = ((pr[0] + pr[1]) + (pr[2] + pr[3])) + ((pr[4] + pr[5]) + (pr[6] + pr[7]));
    { const float t = tsum32(l, l); l = asum16(t); }
    if (lane < 4) *(LAS f2*)(L + L_AML + (w * 4 + lane) * 8) = f2{ms, l};
    if (g == 0) {
#pragma unroll
      for (int h = 0; h < 4; ++h) {
        *(LAS f4*)(L + L_AO + (((w * 4 + h) * 2 + 0) * 16 + j) * 16) = f4{o[0][h], o[1][h], o[2][h], o[3][h]};
        *(LAS f4*)(L + L_AO + (((w * 4 + h) * 2 + 1) * 16 + j) * 16) = f4{o[4][h], o[5][h], o[6][h], o[7][h]};
      }
    }
    bar();
    f2 ml[4], ov[4];
#pragma unroll
    for (int ww = 0; ww < 4; ++ww) { ml[ww] = *(LAS f2*)(L + L_AML + (ww * 4 + w) * 8);
      ov[ww] = *(LAS f2*)(L + L_AO + (((ww * 4 + w) * 2 + ((lane >> 1) & 1)) * 16 + (lane >> 2)) * 16 + (lane & 1) * 8); }
    const float M = vmax(vmax3(ml[0].x, ml[1].x, ml[2].x), ml[3].x);
    float o0 = 0.f, o1 = 0.f;
#pragma unroll
    for (int ww = 0; ww < 4; ++ww) { const float e = __builtin_amdgcn_exp2f(ml[ww].x - M); o0 = fmaf(e, ov[ww].x, o0); o1 = fmaf(e, ov[ww].y, o1); }
    st_sc1_32(c.ws, WS_OB + LAYER * OB_L + ((c.bh * 8 + c.s) * 4 + w) * 256 + lane * 4, nosent(pk_bf16(o0, o1)));
    if (w == 0) {
      f2 hm[4];
#pragma unroll
      for (int ww = 0; ww < 4; ++ww) hm[ww] = *(LAS f2*)(L + L_AML + (ww * 4 + (lane & 3)) * 8);
      const float Mh = vmax(vmax3(hm[0].x, hm[1].x, hm[2].x), hm[3].x);
      float ls = 0.f;
#pragma unroll
      for (int ww = 0; ww < 4; ++ww) ls = fmaf(__builtin_amdgcn_exp2f(hm[ww].x - Mh), hm[ww].y, ls);
      if (lane < 4) st_sc1_64(c.ws, WS_ML + LAYER * ML_L + (c.bh * 8 + c.s) * 128 + lane * 8, v2u{nosent(asu(Mh)), nosent(asu(ls))});
    }
  }
  STAMP(LAYER * 12 + 3);
  v4u W1raw[8], W2raw[8];
  {
    const int obase = WS_OB + LAYER * OB_L + ((c.bh * 8 + (j & 7)) * 4 + w) * 256 + (32 * (j >> 3) + 8 * g) * 2;
    const int mlbase = WS_ML + LAYER * ML_L + (c.bh * 8 + 4 * (g & 1)) * 128 + w * 8;
    if (LAYER == 0) {
#pragma unroll
      for (int st = 0; st < 4; ++st) transpose84(Woraw[st], &Wo[st * 4]);
      SB();
    } else delay<DLY_B>();
    v4u X0 = ld_sc1(c.ws, obase), X1 = ld_sc1(c.ws, obase + 128);
    v2u ml[4];
#pragma unroll
    for (int i = 0; i < 4; ++i) ml[i] = ld_sc1_64(c.ws, mlbase + i * 128);
    if (LAYER == 0) {
      SB();
      const int ccl = c.cc < 56 ? c.cc : 55;
      const GAS unsigned char* b1 = (const GAS unsigned char*)(p.w1 + (size_t)(128 * w + (lane >> 2)) * FF + 32 * ccl + 8 * (lane & 3));
#pragma unroll
      for (int i = 0; i < 8; ++i) W1raw[i] = *(const GAS v4u*)(b1 + (size_t)(16 * i) * FF * 2);
      const GAS unsigned char* b2 = (const GAS unsigned char*)(p.w2 + (size_t)(32 * ccl + 8 * g) * DM + 128 * w + 8 * j);
#pragma unroll
      for (int rr = 0; rr < 8; ++rr) W2raw[rr] = *(const GAS v4u*)(b2 + rr * DM * 2);
      SB();
    }
#define MLOK (ok2(ml[0]) & ok2(ml[1]) & ok2(ml[2]) & ok2(ml[3]))
    POLL(ok4(X0) & ok4(X1) & MLOK,
         if (!ok4(X0)) X0 = ld_sc1(c.ws, obase); if (!ok4(X1)) X1 = ld_sc1(c.ws, obase + 128);
         if (!MLOK) { ml[0] = ld_sc1_64(c.ws, mlbase); ml[1] = ld_sc1_64(c.ws, mlbase + 128); ml[2] = ld_sc1_64(c.ws, mlbase + 256); ml[3] = ld_sc1_64(c.ws, mlbase + 384); });
    STAMP(LAYER * 12 + 4);
    v4u A1, A3;
#pragma unroll
    for (int e = 0; e < 4; ++e) { A1[e] = dppu<ROR8>(X0[e]); A3[e] = dppu<ROR8>(X1[e]); }
    f4 acc[4];
#pragma unroll
    for (int t = 0; t < 4; ++t) { acc[t] = mfma(X0, Wo[0 * 4 + t], f4{0.f, 0.f, 0.f, 0.f}); }
#pragma unroll
    for (int t = 0; t < 4; ++t) { acc[t] = mfma(X1, Wo[2 * 4 + t], acc[t]); }
#pragma unroll
    for (int t = 0; t < 4; ++t) { acc[t] = mfma(A1, Wo[1 * 4 + t], acc[t]); }
#pragma unroll
    for (int t = 0; t < 4; ++t) { acc[t] = mfma(A3, Wo[3 * 4 + t], acc[t]); }
    const float M = amax16(vmax(vmax3(asf(ml[0].x), asf(ml[1].x), asf(ml[2].x)), asf(ml[3].x)));
    float e[4], Ls = 0.f;
#pragma unroll
    for (int i = 0; i < 4; ++i) { e[i] = __builtin_amdgcn_exp2f(asf(ml[i].x) - M); Ls = fmaf(e[i], asf(ml[i].y), Ls); }
    Ls = asum16(Ls);
    const float inv = __builtin_amdgcn_rcpf(Ls);
#pragma unroll
    for (int i = 0; i < 4; ++i) e[i] *= inv;
    float v[4];
#pragma unroll
    for (int t = 0; t < 4; ++t) v[t] = fmaf(e[0], acc[t][0], e[1] * acc[t][1]) + fmaf(e[2], acc[t][2], e[3] * acc[t][3]);
    const float xx = tsum16(v[0], v[1]), yy = tsum16(v[2], v[3]);
    if (lane < 32) { *(LAS float*)(L + L_RED + (w * 64 + 4 * j + g) * 4) = xx; *(LAS float*)(L + L_RED + (w * 64 + 4 * j + 2 + g) * 4) = yy; }
    bar();
    if (w == 0) {
      float d = 0.f;
#pragma unroll
      for (int ww = 0; ww < 4; ++ww) d += *(LAS float*)(L + L_RED + (ww * 64 + lane) * 4);
      const u32 pk = nosent(pk_bf16(d, dppf<XOR1>(d)));
      if (!(lane & 1)) st_sc1_32(c.ws, WS_CB + LAYER * CB_L + (c.bh * 512 + 64 * c.s + lane) * 2, pk);
    }
  }
  STAMP(LAYER * 12 + 5);
  {
    const int cbase = WS_CB + LAYER * CB_L + ((c.b * 8 + g) * 512 + 128 * w + 8 * j) * 2;
    if (LAYER == 0) {
#pragma unroll
      for (int i = 0; i < 8; ++i) *(LAS v4u*)(L + L_W1 + (128 * w + 16 * i + (lane >> 2)) * 64 + (lane & 3) * 16) = W1raw[i];
      transpose8(W2raw, W2);
      bar();
      const int q4 = j >> 2, pp = j & 3;
#pragma unroll
      for (int f = 0; f < 8; ++f) {
        LAS unsigned char* a0 = L + L_W1 + (64 * f + 32 * (pp >> 1) + 8 * g + q4) * 64 + (8 * w + 4 * (pp & 1)) * 2;
        W1[f] = tr_frag(a0, a0 + 4 * 64);
      }
      const v4u ones = {0x3F803F80u, 0x3F803F80u, 0x3F803F80u, 0x3F803F80u};
      f4 a1 = {0.f, 0.f, 0.f, 0.f};
#pragma unroll
      for (int f = 0; f < 8; ++f) a1 = mfma(ones, W1[f], a1);
      cs1 = a1[0] + dppf<ROR8>(a1[0]);
      SB();
    } else delay<DLY_C>();
    v4u X0 = ld_sc1(c.ws, cbase), X1 = ld_sc1(c.ws, cbase + 4 * 1024);
    const u32 xres = *(LAS u32*)(L + L_XS + (128 * w + 8 * j + 4 * (g >> 1) + 2 * (g & 1)) * 2);
    POLL(ok4(X0) & ok4(X1), if (!ok4(X0)) X0 = ld_sc1(c.ws, cbase); if (!ok4(X1)) X1 = ld_sc1(c.ws, cbase + 4 * 1024));
    STAMP(LAYER * 12 + 6);
    float v[8];
#pragma unroll
    for (int e = 0; e < 4; ++e) { v[2 * e] = bf_lo(X0[e]) + bf_lo(X1[e]); v[2 * e + 1] = bf_hi(X0[e]) + bf_hi(X1[e]); }
    float y[4];
#pragma unroll
    for (int e = 0; e < 4; ++e) y[e] = tsum32(v[e], v[e + 4]);
    const float z0 = tsum16(y[0], y[2]), z1 = tsum16(y[1], y[3]);
    const u32 at = pk_bf16(z0, z1);
    const u32 x1 = pk_bf16(bf_lo(xres) + bf_lo(at), bf_hi(xres) + bf_hi(at));
    *(LAS u32*)(L + L_X1 + (128 * w + 8 * j + 4 * (g >> 1) + 2 * (g & 1)) * 2) = x1;
    const float a = bf_lo(x1), d = bf_hi(x1);
    float s1 = a + d, s2 = fmaf(a, a, d * d);
    { float t = tsum32(s1, s2); t = asum16(t); t += dppf<XOR1>(t); t += dppf<XOR2>(t); t += dppf<ROR4>(t); t += dppf<ROR8>(t); s1 = t; }
    if ((lane & 31) == 0) *(LAS float*)(L + L_ST + w * 8 + (lane >> 5) * 4) = s1;
    bar();
    if (c.cc < 56) {
      f4 acc = {0.f, 0.f, 0.f, 0.f};
#pragma unroll
      for (int f = 0; f < 8; ++f) { const v4u A = *(LAS v4u*)(L + L_X1 + (64 * f + 32 * (j & 1) + 8 * g) * 2); acc = mfma(A, W1[f], acc); }
      f2 st = {0.f, 0.f};
#pragma unroll
      for (int ww = 0; ww < 4; ++ww) st += *(LAS f2*)(L + L_ST + ww * 8);
      const float mean = st.x * (1.f / DM), var = fmaxf(st.y * (1.f / DM) - mean * mean, 0.f), rstd = __builtin_amdgcn_rsqf(var + 1e-5f);
      float val = j < 8 ? acc[0] : acc[1];
      val += dppf<ROR8>(val);
      const float hp = rbf(rstd * (val - mean * cs1));
      const float hv = hp * __builtin_amdgcn_rcpf(1.f + __builtin_amdgcn_exp2f(-1.4426950408889634f * hp));
      const float hn = dppf<XOR1>(hv);
      if (lane < 8 && !(lane & 1)) *(LAS u32*)(L + L_H + (8 * w + lane) * 2) = pk_bf16(hv, hn);
    }
    bar();
    if (c.cc < 56) {
      const v4u A = *(LAS v4u*)(L + L_H + g * 16);
      f4 o[8];
#pragma unroll
      for (int t = 0; t < 8; ++t) o[t] = mfma(A, W2[t], f4{0.f, 0.f, 0.f, 0.f});
      const v4u sv = {nosent(pk_bf16(o[0][0], o[1][0])), nosent(pk_bf16(o[2][0], o[3][0])), nosent(pk_bf16(o[4][0], o[5][0])), nosent(pk_bf16(o[6][0], o[7][0]))};
      if (g == 0) st_sc1(c.ws, WS_DB + LAYER * DB_L + ((c.b * 8 + 2 * w + (j >> 3)) * 56 + c.cc) * 128 + (j & 7) * 16, sv);
    }
  }
  STAMP(LAYER * 12 + 7);
  if (c.hkv == c.s) {
    const int dbase = WS_DB + LAYER * DB_L + ((c.b * 8 + c.s) * 56 + 16 * w + (lane >> 3)) * 128 + (lane & 7) * 16;
    delay<DLY_D>();
    v4u X0 = ld_sc1(c.ws, dbase), X1 = {0u, 0u, 0u, 0u};
    if (w < 3) X1 = ld_sc1(c.ws, dbase + 1024);
    POLL(ok4(X0) & ok4(X1), if (!ok4(X0)) X0 = ld_sc1(c.ws, dbase); if (!ok4(X1)) X1 = ld_sc1(c.ws, dbase + 1024));
    STAMP(LAYER * 12 + 8);
    float v[8];
#pragma unroll
    for (int e = 0; e < 4; ++e) { v[2 * e] = bf_lo(X0[e]) + bf_lo(X1[e]); v[2 * e + 1] = bf_hi(X0[e]) + bf_hi(X1[e]); }
    float y[4];
#pragma unroll
    for (int e = 0; e < 4; ++e) y[e] = tsum32(v[e], v[e + 4]);
    float z0 = tsum16(y[0], y[2]), z1 = tsum16(y[1], y[3]);
    z0 += dppf<ROR8>(z0); z1 += dppf<ROR8>(z1);
    if (!(lane & 8)) *(LAS f2*)(L + L_RED + (w * 64 + 8 * (lane & 7) + 4 * (g >> 1) + 2 * (g & 1)) * 4) = f2{z0, z1};
    bar();
    if (w == 0) {
      float d = 0.f;
#pragma unroll
      for (int ww = 0; ww < 4; ++ww) d += *(LAS float*)(L + L_RED + (ww * 64 + lane) * 4);
      const u16 x1h = *(LAS u16*)(L + L_X1 + (64 * c.s + lane) * 2);
      const float x2 = bf_lo((u32)x1h) + rbf(d);
      const u32 pk = pk_bf16(x2, dppf<XOR1>(x2));
      if (!(lane & 1)) {
        const int off = c.b * 1024 + (64 * c.s + lane) * 2;
        if (LAYER == 0) st_sc1_32(c.ws, WS_EB + off, nosent(pk));
        else *(GAS u32*)((GAS unsigned char*)p.out + off) = pk;
      }
    }
    if (LAYER == 0) bar();
  }
  STAMP(LAYER * 12 + 9);
}

__global__ void __launch_bounds__(256, 1) fused_decode(Params p) {
  extern __shared__ __attribute__((aligned(16))) unsigned char smem[];
  Ctx c;
  c.L = (LAS unsigned char*)smem;
  c.tid = threadIdx.x; c.lane = c.tid & 63; c.w = __builtin_amdgcn_readfirstlane(c.tid >> 6); c.j = c.lane & 15; c.g = c.lane >> 4;
  const int tid = c.tid, lane = c.lane, w = c.w, j = c.j, g = c.g;
  c.hkv = blockIdx.x & 7; c.b = blockIdx.x >> 6; c.s = (blockIdx.x >> 3) & 7; c.cc = c.hkv + 8 * c.s; c.bh = c.b * 8 + c.hkv;
  c.ws = __builtin_amdgcn_make_buffer_rsrc(p.ws, 0, WS_END, 0x00020000);
  u32 stamps[32];
  STAMP(0);
  const v4u sent = {SENT, SENT, SENT, SENT};
  st_sc1(c.ws, WS_SCRUB + blockIdx.x * SCRUB_PER + tid * 16, sent);
  if (tid < (SCRUB_PER - 4096) / 16) st_sc1(c.ws, WS_SCRUB + blockIdx.x * SCRUB_PER + 4096 + tid * 16, sent);
  const u64 E = *(const GAS u64*)(p.ws + WS_EPOCH);
  c.flagv = E + 1; c.tag = (u32)c.flagv;
  const GAS unsigned char* xb = (const GAS unsigned char*)p.x;
  v4u xa = *(const GAS v4u*)(xb + c.b * 1024 + (128 * w + 32 * (j & 3) + 8 * g) * 2);
  v2u Wqraw[4][8];
  const GAS unsigned char* qb = (const GAS unsigned char*)(p.wq + (size_t)(128 * w + 8 * g) * NQ + c.hkv * 512 + 64 * c.s + 4 * j);
#pragma unroll
  for (int st = 0; st < 4; ++st)
#pragma unroll
    for (int rr = 0; rr < 8; ++rr) Wqraw[st][rr] = *(const GAS v2u*)(qb + (size_t)(32 * st + rr) * NQ * 2);
  v4u Kf[8];
  const GAS unsigned char* kb = (const GAS unsigned char*)(p.kc + ((size_t)c.bh * SK + (SK - WIN) + c.s * 128 + 32 * w + j) * DH + 8 * g);
#pragma unroll
  for (int kt = 0; kt < 2; ++kt)
#pragma unroll
    for (int st = 0; st < 4; ++st) Kf[kt * 4 + st] = *(const GAS v4u*)(kb + (16 * kt * DH + 32 * st) * 2);
  SB();
  STAMP(10);
  v4u Wq[16], Vf[8], Wo[16], W1[8], W2[8];
#pragma unroll
  for (int st = 0; st < 4; ++st) transpose84(Wqraw[st], &Wq[st * 4]);
  STAMP(11);
  float csq = 0.f, cs1 = 0.f;
  layer<0>(p, c, Wq, Kf, Vf, Wo, W1, W2, csq, cs1, xa, stamps);
  layer<1>(p, c, Wq, Kf, Vf, Wo, W1, W2, csq, cs1, xa, stamps);
  STAMP(23);
}

extern "C" void kernel_launch(void* const* d_in, const int* in_sizes, int n_in, void* d_out, int out_size, void* d_ws, size_t ws_size, hipStream_t stream) {
  Params p;
  p.x = (const u16*)d_in[0]; p.kc = (const u16*)d_in[1]; p.vc = (const u16*)d_in[2]; p.wq = (const u16*)d_in[3]; p.wo = (const u16*)d_in[4];
  p.w1 = (const u16*)d_in[6]; p.w2 = (const u16*)d_in[7]; p.out = (u16*)d_out; p.ws = (unsigned char*)d_ws;
  (void)hipFuncSetAttribute((const void*)fused_decode, hipFuncAttributeMaxDynamicSharedMemorySize, LDS_BYTES);
  hipLaunchKernelGGL(fused_decode, dim3(256), dim3(256), LDS_BYTES, stream, p);
}
```

```cpp
#include <hip/hip_runtime.h>
#include <hip/hip_bf16.h>

typedef unsigned short u16;
constexpr int WS_QG = 8192, WS_OB = 73728, OB_REC = 384, WS_CB = 860160, WS_DB = 925696, WS_EB = 1384448, WS_QD = 1388544;
constexpr int LDS_BYTES = 86016;

struct Params { const u16* x; const u16* kc; const u16* vc; const u16* wq; const u16* wo; const u16* w1; const u16* w2; u16* out; unsigned char* ws; };

__global__ void __launch_bounds__(256, 1) fused_decode(Params p) {
  const unsigned long long kp = (unsigned long long)__builtin_amdgcn_kernarg_segment_ptr();
  const unsigned wg = blockIdx.x, tid = threadIdx.x;
  asm volatile(
      "s_load_dwordx16 s[84:99], %[kp], 0x0\n"
      "s_load_dwordx2 s[82:83], %[kp], 0x40\n"
      "s_mov_b32 s3, %[wg]\n"
      "v_mov_b32 v0, %[tid]\n"
      "v_and_b32 v1, 63, v0\n"
      "v_and_b32 v2, 15, v0\n"
      "v_bfe_u32 v3, v0, 4, 2\n"
      "v_lshrrev_b32 v16, 6, v0\n"
      "s_nop 0\n"
      "v_readfirstlane_b32 s40, v16\n"
      "s_and_b32 s41, s3, 7\n"
      "s_bfe_u32 s42, s3, 0x30003\n"
      "s_lshr_b32 s43, s3, 6\n"
      "s_lshl_b32 s44, s43, 3\n"
      "s_or_b32 s44, s44, s41\n"
      "s_lshl_b32 s45, s42, 3\n"
      "s_or_b32 s45, s45, s41\n"
      "s_min_u32 s46, s45, 55\n"
      "s_cmp_eq_u32 s41, s42\n"
      "s_cselect_b32 s47, 1, 0\n"
      "s_mov_b32 s50, 0x05040100\n"
      "s_mov_b32 s51, 0x07060302\n"
      "v_lshrrev_b32 v16, 3, v2\n"
      "v_lshlrev_b32 v16, 7, v16\n"
      "v_bfe_u32 v17, v2, 2, 1\n"
      "v_lshl_add_u32 v16, v17, 4, v16\n"
      "v_lshl_add_u32 v16, v3, 5, v16\n"
      "s_lshl_b32 s52, s40, 8\n"
      "v_add_u32 v10, s52, v16\n"
      "v_lshlrev_b32 v24, 4, v1\n"
      "v_lshrrev_b32 v25, 3, v1\n"
      "v_lshlrev_b32 v25, 16, v25\n"
      "v_and_b32 v26, 7, v1\n"
      "v_lshl_add_u32 v25, v26, 4, v25\n"
      "s_lshl_b32 s52, s40, 20\n"
      "s_lshl_b32 s53, s41, 10\n"
      "s_add_u32 s52, s52, s53\n"
      "s_lshl_b32 s53, s42, 7\n"
      "s_add_u32 s52, s52, s53\n"
      "v_add_u32 v25, s52, v25\n"
      "s_lshl_b32 s53, s43, 10\n"
      "s_waitcnt lgkmcnt(0)\n"
      "s_mov_b32 s76, s84\n"
      "s_and_b32 s77, s85, 0xffff\n"
      "s_mov_b32 s78, 4096\n"
      "s_mov_b32 s79, 0x20000\n"
      "buffer_load_dwordx4 v[16:19], v10, s[76:79], s53 offen\n"
      "buffer_load_dwordx4 v[20:23], v24, s[76:79], s53 offen\n"
      "s_mov_b32 s8, s90\n"
      "s_and_b32 s9, s91, 0xffff\n"
      "s_mov_b32 s10, 4194304\n"
      "s_mov_b32 s11, 0x20000\n"
      "buffer_load_dwordx4 v[64:67], v25, s[8:11], 0 offen\n"
      "s_mov_b32 s58, 0x2000\n"
      "buffer_load_dwordx4 v[68:71], v25, s[8:11], s58 offen\n"
      "s_mov_b32 s58, 0x4000\n"
      "buffer_load_dwordx4 v[72:75], v25, s[8:11], s58 offen\n"
      "s_mov_b32 s58, 0x6000\n"
      "buffer_load_dwordx4 v[76:79], v25, s[8:11], s58 offen\n"
      "s_mov_b32 s58, 0x8000\n"
      "buffer_load_dwordx4 v[80:83], v25, s[8:11], s58 offen\n"
      "s_mov_b32 s58, 0xa000\n"
      "buffer_load_dwordx4 v[84:87], v25, s[8:11], s58 offen\n"
      "s_mov_b32 s58, 0xc000\n"
      "buffer_load_dwordx4 v[88:91], v25, s[8:11], s58 offen\n"
      "s_mov_b32 s58, 0xe000\n"
      "buffer_load_dwordx4 v[92:95], v25, s[8:11], s58 offen\n"
      "s_mov_b32 s58, 0x80000\n"
      "buffer_load_dwordx4 v[96:99], v25, s[8:11], s58 offen\n"
      "s_mov_b32 s58, 0x82000\n"
      "buffer_load_dwordx4 v[100:103], v25, s[8:11], s58 offen\n"
      "s_mov_b32 s58, 0x84000\n"
      "buffer_load_dwordx4 v[104:107], v25, s[8:11], s58 offen\n"
      "s_mov_b32 s58, 0x86000\n"
      "buffer_load_dwordx4 v[108:111], v25, s[8:11], s58 offen\n"
      "s_mov_b32 s58, 0x88000\n"
      "buffer_load_dwordx4 v[112:115], v25, s[8:11], s58 offen\n"
      "s_mov_b32 s58, 0x8a000\n"
      "buffer_load_dwordx4 v[116:119], v25, s[8:11], s58 offen\n"
      "s_mov_b32 s58, 0x8c000\n"
      "buffer_load_dwordx4 v[120:123], v25, s[8:11], s58 offen\n"
      "s_mov_b32 s58, 0x8e000\n"
      "buffer_load_dwordx4 v[124:127], v25, s[8:11], s58 offen\n"
      "s_load_dwordx2 s[60:61], s[82:83], 0x0\n"
      "s_mov_b32 s4, s82\n"
      "s_and_b32 s5, s83, 0xffff\n"
      "s_mov_b32 s6, 1449984\n"
      "s_mov_b32 s7, 0x20000\n"
      "s_mov_b32 s12, s92\n"
      "s_and_b32 s13, s93, 0xffff\n"
      "s_mov_b32 s14, 4194304\n"
      "s_mov_b32 s15, 0x20000\n"
      "s_mov_b32 s16, s94\n"
      "s_and_b32 s17, s95, 0xffff\n"
      "s_mov_b32 s18, 1835008\n"
      "s_mov_b32 s19, 0x20000\n"
      "s_mov_b32 s20, s96\n"
      "s_and_b32 s21, s97, 0xffff\n"
      "s_mov_b32 s22, 1835008\n"
      "s_mov_b32 s23, 0x20000\n"
      "s_mov_b32 s36, s98\n"
      "s_and_b32 s37, s99, 0xffff\n"
      "s_mov_b32 s38, 4096\n"
      "s_mov_b32 s39, 0x20000\n"
      "s_lshl_b32 s52, s44, 20\n"
      "s_lshl_b32 s53, s42, 15\n"
      "s_add_u32 s52, s52, s53\n"
      "s_add_u32 s52, s52, 786432\n"
      "s_add_u32 s86, s86, s52\n"
      "s_addc_u32 s87, s87, 0\n"
      "s_add_u32 s88, s88, s52\n"
      "s_addc_u32 s89, s89, 0\n"
      "s_mov_b32 s24, s86\n"
      "s_and_b32 s25, s87, 0xffff\n"
      "s_mov_b32 s26, 32768\n"
      "s_mov_b32 s27, 0x20000\n"
      "s_mov_b32 s28, s88\n"
      "s_and_b32 s29, s89, 0xffff\n"
      "s_mov_b32 s30, 32768\n"
      "s_mov_b32 s31, 0x20000\n"
      "v_mov_b32 v26, 0\n"
      "v_mov_b32 v27, 4352\n"
      "ds_write_b32 v27, v26\n"
      "s_waitcnt lgkmcnt(0)\n"
      "s_barrier\n"
      "v_mov_b32 v28, -1\n"
      "v_mov_b32 v29, -1\n"
      "v_mov_b32 v30, -1\n"
      "v_mov_b32 v31, -1\n"
      "s_mul_i32 s52, s3, 5376\n"
      "s_add_u32 s52, s52, 73728\n"
      "v_lshlrev_b32 v26, 4, v0\n"
      "buffer_store_dwordx4 v[28:31], v26, s[4:7], s52 offen sc1\n"
      "s_mul_i32 s53, s40, 320\n"
      "s_add_u32 s53, s53, s52\n"
      "s_add_u32 s53, s53, 4096\n"
      "s_mov_b64 exec, 0xfffff\n"
      "buffer_store_dwordx4 v[28:31], v24, s[4:7], s53 offen sc1\n"
      "s_mov_b64 exec, -1\n"
      "v_lshlrev_b32 v26, 8, v2\n"
      "v_lshl_add_u32 v26, v3, 4, v26\n"
      "s_lshl_b32 s52, s40, 13\n"
      "s_add_u32 s53, s52, 4096\n"
      "buffer_load_dwordx4 a[0:3], v26, s[24:27], s52 offen offset:0 nt\n"
      "buffer_load_dwordx4 a[4:7], v26, s[24:27], s52 offen offset:64 nt\n"
      "buffer_load_dwordx4 a[8:11], v26, s[24:27], s52 offen offset:128 nt\n"
      "buffer_load_dwordx4 a[12:15], v26, s[24:27], s52 offen offset:192 nt\n"
      "buffer_load_dwordx4 a[16:19], v26, s[24:27], s53 offen offset:0 nt\n"
      "buffer_load_dwordx4 a[20:23], v26, s[24:27], s53 offen offset:64 nt\n"
      "buffer_load_dwordx4 a[24:27], v26, s[24:27], s53 offen offset:128 nt\n"
      "buffer_load_dwordx4 a[28:31], v26, s[24:27], s53 offen offset:192 nt\n"
      "s_mov_b32 s86, 0x55555555\n"
      "s_mov_b32 s87, 0x55555555\n"
      "s_mov_b32 s88, 0xff0000ff\n"
      "s_mov_b32 s89, 0x0\n"
      "s_mov_b32 s90, 0x11111111\n"
      "s_mov_b32 s91, 0x11111111\n"
      "s_mov_b32 s92, 0x1\n"
      "s_mov_b32 s93, 0x1\n"
      "s_mov_b32 s94, 0xffffffff\n"
      "s_mov_b32 s95, 0x0\n"
      "s_mov_b32 s96, 0xff00ff\n"
      "s_mov_b32 s97, 0xff00ff\n"
      "s_mov_b32 s98, 0x3b000000\n"
      "s_mov_b32 s99, 0x3b000000\n"
      "s_mov_b32 s63, 0x3e0293ee\n"
      "v_and_b32 v26, 7, v2\n"
      "v_lshrrev_b32 v27, 1, v26\n"
      "v_and_b32 v26, 1, v26\n"
      "v_lshlrev_b32 v26, 4, v26\n"
      "v_mov_b32 v28, 0x3f80\n"
      "v_lshlrev_b32 v26, v26, v28\n"
      "v_cmp_eq_u32 vcc, 0, v27\n"
      "s_nop 1\n"
      "v_cndmask_b32 v6, 0, v26, vcc\n"
      "v_cmp_eq_u32 vcc, 1, v27\n"
      "s_nop 1\n"
      "v_cndmask_b32 v7, 0, v26, vcc\n"
      "v_cmp_eq_u32 vcc, 2, v27\n"
      "s_nop 1\n"
      "v_cndmask_b32 v8, 0, v26, vcc\n"
      "v_cmp_eq_u32 vcc, 3, v27\n"
      "s_nop 1\n"
      "v_cndmask_b32 v9, 0, v26, vcc\n"
      "s_waitcnt vmcnt(26)\n"
      "s_mov_b64 exec, s[90:91]\n"
      "ds_write_b128 v10, v[16:19] offset:5632\n"
      "s_mov_b64 exec, -1\n"
      "v_mov_b32 v24, 0\n"
      "v_mov_b32 v25, 0\n"
      "v_lshlrev_b32 v26, 16, v20\n"
      "v_and_b32 v27, 0xffff0000, v20\n"
      "v_add_f32 v24, v24, v26\n"
      "v_fmac_f32 v25, v26, v26\n"
      "v_add_f32 v24, v24, v27\n"
      "v_fmac_f32 v25, v27, v27\n"
      "v_lshlrev_b32 v26, 16, v21\n"
      "v_and_b32 v27, 0xffff0000, v21\n"
      "v_add_f32 v24, v24, v26\n"
      "v_fmac_f32 v25, v26, v26\n"
      "v_add_f32 v24, v24, v27\n"
      "v_fmac_f32 v25, v27, v27\n"
      "v_lshlrev_b32 v26, 16, v22\n"
      "v_and_b32 v27, 0xffff0000, v22\n"
      "v_add_f32 v24, v24, v26\n"
      "v_fmac_f32 v25, v26, v26\n"
      "v_add_f32 v24, v24, v27\n"
      "v_fmac_f32 v25, v27, v27\n"
      "v_lshlrev_b32 v26, 16, v23\n"
      "v_and_b32 v27, 0xffff0000, v23\n"
      "v_add_f32 v24, v24, v26\n"
      "v_fmac_f32 v25, v26, v26\n"
      "v_add_f32 v24, v24, v27\n"
      "v_fmac_f32 v25, v27, v27\n"
      "s_nop 0\n"
      "v_add_f32_dpp v24, v24, v24 quad_perm:[1,0,3,2] row_mask:0xf bank_mask:0xf bound_ctrl:1\n"
      "v_add_f32_dpp v25, v25, v25 quad_perm:[1,0,3,2] row_mask:0xf bank_mask:0xf bound_ctrl:1\n"
      "s_nop 0\n"
      "v_add_f32_dpp v24, v24, v24 quad_perm:[2,3,0,1] row_mask:0xf bank_mask:0xf bound_ctrl:1\n"
      "v_add_f32_dpp v25, v25, v25 quad_perm:[2,3,0,1] row_mask:0xf bank_mask:0xf bound_ctrl:1\n"
      "s_nop 0\n"
      "v_add_f32_dpp v24, v24, v24 row_ror:4 row_mask:0xf bank_mask:0xf bound_ctrl:1\n"
      "v_add_f32_dpp v25, v25, v25 row_ror:4 row_mask:0xf bank_mask:0xf bound_ctrl:1\n"
      "s_nop 0\n"
      "v_add_f32_dpp v24, v24, v24 row_ror:8 row_mask:0xf bank_mask:0xf bound_ctrl:1\n"
      "v_add_f32_dpp v25, v25, v25 row_ror:8 row_mask:0xf bank_mask:0xf bound_ctrl:1\n"
      "s_nop 1\n"
      "v_permlane32_swap_b32 v24, v25\n"
      "v_add_f32 v24, v24, v25\n"
      "v_mov_b32 v25, v24\n"
      "s_nop 1\n"
      "v_permlane16_swap_b32 v24, v25\n"
      "v_add_f32 v24, v24, v25\n"
      "v_mov_b32 v25, v24\n"
      "s_nop 1\n"
      "v_permlane32_swap_b32 v24, v25\n"
      "v_mul_f32 v24, 0x3b000000, v24\n"
      "v_mul_f32 v25, 0x3b000000, v25\n"
      "v_fma_f32 v25, -v24, v24, v25\n"
      "v_max_f32 v25, 0, v25\n"
      "v_add_f32 v25, 0x3727c5ac, v25\n"
      "v_rsq_f32 v25, v25\n"
      "v_lshlrev_b32 v26, 16, v16\n"
      "v_and_b32 v27, 0xffff0000, v16\n"
      "v_sub_f32 v26, v26, v24\n"
      "v_sub_f32 v27, v27, v24\n"
      "v_mul_f32 v26, v26, v25\n"
      "v_mul_f32 v27, v27, v25\n"
      "v_cvt_pk_bf16_f32 v16, v26, v27\n"
      "v_lshlrev_b32 v26, 16, v17\n"
      "v_and_b32 v27, 0xffff0000, v17\n"
      "v_sub_f32 v26, v26, v24\n"
      "v_sub_f32 v27, v27, v24\n"
      "v_mul_f32 v26, v26, v25\n"
      "v_mul_f32 v27, v27, v25\n"
      "v_cvt_pk_bf16_f32 v17, v26, v27\n"
      "v_lshlrev_b32 v26, 16, v18\n"
      "v_and_b32 v27, 0xffff0000, v18\n"
      "v_sub_f32 v26, v26, v24\n"
      "v_sub_f32 v27, v27, v24\n"
      "v_mul_f32 v26, v26, v25\n"
      "v_mul_f32 v27, v27, v25\n"
      "v_cvt_pk_bf16_f32 v18, v26, v27\n"
      "v_lshlrev_b32 v26, 16, v19\n"
      "v_and_b32 v27, 0xffff0000, v19\n"
      "v_sub_f32 v26, v26, v24\n"
      "v_sub_f32 v27, v27, v24\n"
      "v_mul_f32 v26, v26, v25\n"
      "v_mul_f32 v27, v27, v25\n"
      "v_cvt_pk_bf16_f32 v19, v26, v27\n"
      "v_mov_b32_dpp v20, v16 row_ror:8 row_mask:0xf bank_mask:0xf bound_ctrl:1\n"
      "v_mov_b32_dpp v21, v17 row_ror:8 row_mask:0xf bank_mask:0xf bound_ctrl:1\n"
      "v_mov_b32_dpp v22, v18 row_ror:8 row_mask:0xf bank_mask:0xf bound_ctrl:1\n"
      "v_mov_b32_dpp v23, v19 row_ror:8 row_mask:0xf bank_mask:0xf bound_ctrl:1\n"
      "v_and_b32 v60, 7, v2\n"
      "v_lshlrev_b32 v60, 5, v60\n"
      "v_lshl_add_u32 v60, v3, 8, v60\n"
      "s_lshl_b32 s52, s40, 9\n"
      "v_add_u32 v60, s52, v60\n"
      "v_lshlrev_b32 v61, 2, v1\n"
      "v_bfe_u32 v62, v1, 2, 1\n"
      "v_lshlrev_b32 v62, 7, v62\n"
      "v_lshrrev_b32 v63, 3, v1\n"
      "v_lshl_add_u32 v62, v63, 4, v62\n"
      "v_bfe_u32 v63, v1, 1, 1\n"
      "v_lshl_add_u32 v62, v63, 3, v62\n"
      "s_lshl_b32 s53, s44, 3\n"
      "s_add_u32 s53, s53, s42\n"
      "s_lshl_b32 s62, s53, 8\n"
      "s_add_u32 s62, s62, 8192\n"
      "s_waitcnt vmcnt(18)\n"
      "v_perm_b32 v128, v68, v64, s50\n"
      "v_perm_b32 v129, v76, v72, s50\n"
      "v_perm_b32 v130, v84, v80, s50\n"
      "v_perm_b32 v131, v92, v88, s50\n"
      "v_perm_b32 v132, v68, v64, s51\n"
      "v_perm_b32 v133, v76, v72, s51\n"
      "v_perm_b32 v134, v84, v80, s51\n"
      "v_perm_b32 v135, v92, v88, s51\n"
      "v_perm_b32 v136, v69, v65, s50\n"
      "v_perm_b32 v137, v77, v73, s50\n"
      "v_perm_b32 v138, v85, v81, s50\n"
      "v_perm_b32 v139, v93, v89, s50\n"
      "v_perm_b32 v140, v69, v65, s51\n"
      "v_perm_b32 v141, v77, v73, s51\n"
      "v_perm_b32 v142, v85, v81, s51\n"
      "v_perm_b32 v143, v93, v89, s51\n"
      "v_perm_b32 v144, v70, v66, s50\n"
      "v_perm_b32 v145, v78, v74, s50\n"
      "v_perm_b32 v146, v86, v82, s50\n"
      "v_perm_b32 v147, v94, v90, s50\n"
      "v_perm_b32 v148, v70, v66, s51\n"
      "v_perm_b32 v149, v78, v74, s51\n"
      "v_perm_b32 v150, v86, v82, s51\n"
      "v_perm_b32 v151, v94, v90, s51\n"
      "v_perm_b32 v152, v71, v67, s50\n"
      "v_perm_b32 v153, v79, v75, s50\n"
      "v_perm_b32 v154, v87, v83, s50\n"
      "v_perm_b32 v155, v95, v91, s50\n"
      "v_perm_b32 v156, v71, v67, s51\n"
      "v_perm_b32 v157, v79, v75, s51\n"
      "v_perm_b32 v158, v87, v83, s51\n"
      "v_perm_b32 v159, v95, v91, s51\n"
      "v_mfma_f32_16x16x32_bf16 v[24:27], v[16:19], v[128:131], 0\n"
      "v_mfma_f32_16x16x32_bf16 v[28:31], v[16:19], v[132:135], 0\n"
      "v_mfma_f32_16x16x32_bf16 v[32:35], v[16:19], v[136:139], 0\n"
      "v_mfma_f32_16x16x32_bf16 v[36:39], v[16:19], v[140:143], 0\n"
      "v_mfma_f32_16x16x32_bf16 v[40:43], v[16:19], v[144:147], 0\n"
      "v_mfma_f32_16x16x32_bf16 v[44:47], v[16:19], v[148:151], 0\n"
      "v_mfma_f32_16x16x32_bf16 v[48:51], v[16:19], v[152:155], 0\n"
      "v_mfma_f32_16x16x32_bf16 v[52:55], v[16:19], v[156:159], 0\n"
      "s_waitcnt vmcnt(10)\n"
      "v_perm_b32 v160, v100, v96, s50\n"
      "v_perm_b32 v161, v108, v104, s50\n"
      "v_perm_b32 v162, v116, v112, s50\n"
      "v_perm_b32 v163, v124, v120, s50\n"
      "v_perm_b32 v164, v100, v96, s51\n"
      "v_perm_b32 v165, v108, v104, s51\n"
      "v_perm_b32 v166, v116, v112, s51\n"
      "v_perm_b32 v167, v124, v120, s51\n"
      "v_perm_b32 v168, v101, v97, s50\n"
      "v_perm_b32 v169, v109, v105, s50\n"
      "v_perm_b32 v170, v117, v113, s50\n"
      "v_perm_b32 v171, v125, v121, s50\n"
      "v_perm_b32 v172, v101, v97, s51\n"
      "v_perm_b32 v173, v109, v105, s51\n"
      "v_perm_b32 v174, v117, v113, s51\n"
      "v_perm_b32 v175, v125, v121, s51\n"
      "v_perm_b32 v176, v102, v98, s50\n"
      "v_perm_b32 v177, v110, v106, s50\n"
      "v_perm_b32 v178, v118, v114, s50\n"
      "v_perm_b32 v179, v126, v122, s50\n"
      "v_perm_b32 v180, v102, v98, s51\n"
      "v_perm_b32 v181, v110, v106, s51\n"
      "v_perm_b32 v182, v118, v114, s51\n"
      "v_perm_b32 v183, v126, v122, s51\n"
      "v_perm_b32 v184, v103, v99, s50\n"
      "v_perm_b32 v185, v111, v107, s50\n"
      "v_perm_b32 v186, v119, v115, s50\n"
      "v_perm_b32 v187, v127, v123, s50\n"
      "v_perm_b32 v188, v103, v99, s51\n"
      "v_perm_b32 v189, v111, v107, s51\n"
      "v_perm_b32 v190, v119, v115, s51\n"
      "v_perm_b32 v191, v127, v123, s51\n"
      "v_mfma_f32_16x16x32_bf16 v[24:27], v[20:23], v[160:163], v[24:27]\n"
      "v_mfma_f32_16x16x32_bf16 v[28:31], v[20:23], v[164:167], v[28:31]\n"
      "v_mfma_f32_16x16x32_bf16 v[32:35], v[20:23], v[168:171], v[32:35]\n"
      "v_mfma_f32_16x16x32_bf16 v[36:39], v[20:23], v[172:175], v[36:39]\n"
      "v_mfma_f32_16x16x32_bf16 v[40:43], v[20:23], v[176:179], v[40:43]\n"
      "v_mfma_f32_16x16x32_bf16 v[44:47], v[20:23], v[180:183], v[44:47]\n"
      "v_mfma_f32_16x16x32_bf16 v[48:51], v[20:23], v[184:187], v[48:51]\n"
      "v_mfma_f32_16x16x32_bf16 v[52:55], v[20:23], v[188:191], v[52:55]\n"
      "s_mov_b64 exec, s[88:89]\n"
      "s_nop 0\n"
      "ds_write2_b32 v60, v24, v28 offset0:0 offset1:1\n"
      "s_nop 0\n"
      "ds_write2_b32 v60, v32, v36 offset0:2 offset1:3\n"
      "s_nop 0\n"
      "ds_write2_b32 v60, v40, v44 offset0:4 offset1:5\n"
      "s_nop 0\n"
      "ds_write2_b32 v60, v48, v52 offset0:6 offset1:7\n"
      "s_mov_b64 exec, -1\n"
      "s_waitcnt lgkmcnt(0)\n"
      "s_barrier\n"
      "s_cmp_lg_u32 s40, 0\n"
      "s_cbranch_scc1 .Lfd_qf_1\n"
      "ds_read2st64_b32 v[16:17], v61 offset0:0 offset1:1\n"
      "ds_read2st64_b32 v[18:19], v61 offset0:2 offset1:3\n"
      "ds_read2st64_b32 v[20:21], v61 offset0:4 offset1:5\n"
      "ds_read2st64_b32 v[22:23], v61 offset0:6 offset1:7\n"
      "s_waitcnt lgkmcnt(0)\n"
      "v_pk_add_f32 v[16:17], v[16:17], v[18:19]\n"
      "v_pk_add_f32 v[20:21], v[20:21], v[22:23]\n"
      "v_pk_add_f32 v[16:17], v[16:17], v[20:21]\n"
      "v_add_f32 v16, v16, v17\n"
      "s_nop 1\n"
      "v_mov_b32_dpp v17, v16 quad_perm:[1,0,3,2] row_mask:0xf bank_mask:0xf bound_ctrl:1\n"
      "v_cvt_pk_bf16_f32 v16, v16, v17\n"
      "s_waitcnt lgkmcnt(0)\n"
      "s_add_u32 s48, s60, 1\n"
      "s_addc_u32 s49, s61, 0\n"
      "v_mov_b32 v17, s48\n"
      "s_mov_b64 exec, s[86:87]\n"
      "buffer_store_dwordx2 v[16:17], v62, s[4:7], s62 offen sc1\n"
      "s_mov_b64 exec, -1\n"
      "s_waitcnt vmcnt(9)\n"
      "s_branch .Lfd_qf2_2\n"
      ".Lfd_qf_1:\n"
      "s_waitcnt lgkmcnt(0)\n"
      "s_add_u32 s48, s60, 1\n"
      "s_addc_u32 s49, s61, 0\n"
      "s_waitcnt vmcnt(8)\n"
      ".Lfd_qf2_2:\n"
      "v_mov_b32 v16, 4352\n"
      "v_mov_b32 v17, 1\n"
      "s_mov_b64 exec, 1\n"
      "ds_add_rtn_u32 v17, v16, v17\n"
      "s_waitcnt lgkmcnt(0)\n"
      "v_cmp_eq_u32 vcc, 3, v17\n"
      "s_cbranch_vccz .Lfd_qf3_3\n"
      "s_lshl_b32 s52, s3, 3\n"
      "s_add_u32 s52, s52, 4096\n"
      "v_mov_b32 v16, s48\n"
      "v_mov_b32 v17, s49\n"
      "v_mov_b32 v18, 0\n"
      "buffer_store_dwordx2 v[16:17], v18, s[4:7], s52 offen sc1\n"
      ".Lfd_qf3_3:\n"
      "s_mov_b64 exec, -1\n"
      "s_cmp_eq_u32 s40, 0\n"
      "s_cbranch_scc0 .Lfd_a0w_4\n"
      "v_lshrrev_b32 v40, 2, v2\n"
      "v_lshl_add_u32 v40, v40, 2, v3\n"
      "v_and_b32 v41, 3, v2\n"
      "v_lshlrev_b32 v41, 1, v41\n"
      "v_lshrrev_b32 v42, 3, v40\n"
      "v_add_u32 v41, v41, v42\n"
      "v_and_b32 v40, 7, v40\n"
      "v_lshlrev_b32 v40, 4, v40\n"
      "v_lshl_add_u32 v40, v41, 8, v40\n"
      "s_lshl_b32 s57, s44, 11\n"
      "s_add_u32 s57, s57, 8192\n"
      "v_mul_u32_u24 v54, 528, v2\n"
      "v_lshl_add_u32 v54, v3, 4, v54\n"
      "s_mul_i32 s52, s40, 2112\n"
      "s_add_u32 s52, s52, 9472\n"
      "v_add_u32 v54, s52, v54\n"
      "v_lshlrev_b32 v55, 4, v1\n"
      "v_lshl_add_u32 v55, v3, 4, v55\n"
      "v_add_u32 v55, 7680, v55\n"
      "v_cmp_gt_u32 vcc, 4, v2\n"
      "s_nop 1\n"
      "v_cndmask_b32 v54, v55, v54, vcc\n"
      "v_lshlrev_b32 v55, 3, v1\n"
      "s_lshl_b32 s52, s40, 5\n"
      "s_add_u32 s52, s52, 4160\n"
      "v_add_u32 v60, s52, v55\n"
      "v_add_u32 v55, 7680, v55\n"
      "v_cmp_gt_u32 vcc, 4, v1\n"
      "s_nop 1\n"
      "v_cndmask_b32 v55, v55, v60, vcc\n"
      "v_lshlrev_b32 v60, 3, v1\n"
      "s_mul_i32 s52, s40, 528\n"
      "v_add_u32 v60, s52, v60\n"
      "s_lshl_b32 s52, s40, 3\n"
      "v_mov_b32 v61, s52\n"
      "v_lshlrev_b32 v62, 2, v1\n"
      "s_lshl_b32 s52, s44, 3\n"
      "s_add_u32 s52, s52, s42\n"
      "s_lshl_b32 s52, s52, 2\n"
      "s_add_u32 s52, s52, s40\n"
      "s_mul_i32 s62, s52, 384\n"
      "s_add_u32 s62, s62, 73728\n"
      "s_sleep 6\n"
      "s_mov_b32 s56, 131072\n"
      ".Lfd_poll_6:\n"
      "buffer_load_dwordx4 v[16:19], v40, s[4:7], s57 offen sc1\n"
      "buffer_load_dwordx4 v[20:23], v40, s[4:7], s57 offen offset:128 sc1\n"
      "s_waitcnt vmcnt(0)\n"
      "v_xor_b32 v41, s48, v17\n"
      "v_xor_b32 v42, s48, v19\n"
      "v_xor_b32 v43, s48, v21\n"
      "v_xor_b32 v44, s48, v23\n"
      "v_or3_b32 v41, v41, v42, v43\n"
      "v_or_b32 v41, v41, v44\n"
      "v_cmp_ne_u32 vcc, 0, v41\n"
      "s_cbranch_vccz .Lfd_done_7\n"
      "s_sub_u32 s56, s56, 1\n"
      "s_cbranch_scc1 .Lfd_done_7\n"
      "s_sleep 1\n"
      "s_branch .Lfd_poll_6\n"
      ".Lfd_done_7:\n"
      "v_mov_b32 v17, v18\n"
      "v_mov_b32 v18, v20\n"
      "v_mov_b32 v19, v22\n"
      "v_lshlrev_b32 v41, 4, v1\n"
      "ds_write_b128 v41, v[16:19] offset:6656\n"
      "s_branch .Lfd_a0j_5\n"
      ".Lfd_a0w_4:\n"
      "v_lshlrev_b32 v41, 4, v1\n"
      "s_sub_u32 s52, s40, 1\n"
      "s_mul_i32 s52, s52, 11264\n"
      "buffer_load_dwordx4 a[128:131], v41, s[28:31], s52 offen offset:0 nt\n"
      "buffer_load_dwordx4 a[132:135], v41, s[28:31], s52 offen offset:1024 nt\n"
      "buffer_load_dwordx4 a[136:139], v41, s[28:31], s52 offen offset:2048 nt\n"
      "buffer_load_dwordx4 a[140:143], v41, s[28:31], s52 offen offset:3072 nt\n"
      "s_add_u32 s52, s52, 4096\n"
      "buffer_load_dwordx4 a[144:147], v41, s[28:31], s52 offen offset:0 nt\n"
      "buffer_load_dwordx4 a[148:151], v41, s[28:31], s52 offen offset:1024 nt\n"
      "buffer_load_dwordx4 a[152:155], v41, s[28:31], s52 offen offset:2048 nt\n"
      "buffer_load_dwordx4 a[156:159], v41, s[28:31], s52 offen offset:3072 nt\n"
      "s_add_u32 s52, s52, 4096\n"
      "buffer_load_dwordx4 a[160:163], v41, s[28:31], s52 offen offset:0 nt\n"
      "buffer_load_dwordx4 a[164:167], v41, s[28:31], s52 offen offset:1024 nt\n"
      "buffer_load_dwordx4 a[168:171], v41, s[28:31], s52 offen offset:2048 nt\n"
      "v_lshlrev_b32 v42, 13, v3\n"
      "v_lshl_add_u32 v42, v2, 3, v42\n"
      "s_lshl_b32 s53, s41, 19\n"
      "s_lshl_b32 s54, s40, 17\n"
      "s_add_u32 s53, s53, s54\n"
      "s_lshl_b32 s54, s42, 7\n"
      "s_add_u32 s53, s53, s54\n"
      "s_add_u32 s58, s53, 0\n"
      "buffer_load_dwordx2 v[64:65], v42, s[12:15], s58 offen offset:0\n"
      "buffer_load_dwordx2 v[66:67], v42, s[12:15], s58 offen offset:1024\n"
      "buffer_load_dwordx2 v[68:69], v42, s[12:15], s58 offen offset:2048\n"
      "buffer_load_dwordx2 v[70:71], v42, s[12:15], s58 offen offset:3072\n"
      "s_add_u32 s58, s53, 4096\n"
      "buffer_load_dwordx2 v[72:73], v42, s[12:15], s58 offen offset:0\n"
      "buffer_load_dwordx2 v[74:75], v42, s[12:15], s58 offen offset:1024\n"
      "buffer_load_dwordx2 v[76:77], v42, s[12:15], s58 offen offset:2048\n"
      "buffer_load_dwordx2 v[78:79], v42, s[12:15], s58 offen offset:3072\n"
      "s_add_u32 s58, s53, 32768\n"
      "buffer_load_dwordx2 v[80:81], v42, s[12:15], s58 offen offset:0\n"
      "buffer_load_dwordx2 v[82:83], v42, s[12:15], s58 offen offset:1024\n"
      "buffer_load_dwordx2 v[84:85], v42, s[12:15], s58 offen offset:2048\n"
      "buffer_load_dwordx2 v[86:87], v42, s[12:15], s58 offen offset:3072\n"
      "s_add_u32 s58, s53, 36864\n"
      "buffer_load_dwordx2 v[88:89], v42, s[12:15], s58 offen offset:0\n"
      "buffer_load_dwordx2 v[90:91], v42, s[12:15], s58 offen offset:1024\n"
      "buffer_load_dwordx2 v[92:93], v42, s[12:15], s58 offen offset:2048\n"
      "buffer_load_dwordx2 v[94:95], v42, s[12:15], s58 offen offset:3072\n"
      "s_add_u32 s58, s53, 65536\n"
      "buffer_load_dwordx2 v[96:97], v42, s[12:15], s58 offen offset:0\n"
      "buffer_load_dwordx2 v[98:99], v42, s[12:15], s58 offen offset:1024\n"
      "buffer_load_dwordx2 v[100:101], v42, s[12:15], s58 offen offset:2048\n"
      "buffer_load_dwordx2 v[102:103], v42, s[12:15], s58 offen offset:3072\n"
      "s_add_u32 s58, s53, 69632\n"
      "buffer_load_dwordx2 v[104:105], v42, s[12:15], s58 offen offset:0\n"
      "buffer_load_dwordx2 v[106:107], v42, s[12:15], s58 offen offset:1024\n"
      "buffer_load_dwordx2 v[108:109], v42, s[12:15], s58 offen offset:2048\n"
      "buffer_load_dwordx2 v[110:111], v42, s[12:15], s58 offen offset:3072\n"
      "s_add_u32 s58, s53, 98304\n"
      "buffer_load_dwordx2 v[112:113], v42, s[12:15], s58 offen offset:0\n"
      "buffer_load_dwordx2 v[114:115], v42, s[12:15], s58 offen offset:1024\n"
      "buffer_load_dwordx2 v[116:117], v42, s[12:15], s58 offen offset:2048\n"
      "buffer_load_dwordx2 v[118:119], v42, s[12:15], s58 offen offset:3072\n"
      "s_add_u32 s58, s53, 102400\n"
      "buffer_load_dwordx2 v[120:121], v42, s[12:15], s58 offen offset:0\n"
      "buffer_load_dwordx2 v[122:123], v42, s[12:15], s58 offen offset:1024\n"
      "buffer_load_dwordx2 v[124:125], v42, s[12:15], s58 offen offset:2048\n"
      "buffer_load_dwordx2 v[126:127], v42, s[12:15], s58 offen offset:3072\n"
      "v_mul_u32_u24 v54, 528, v2\n"
      "v_lshl_add_u32 v54, v3, 4, v54\n"
      "s_mul_i32 s52, s40, 2112\n"
      "s_add_u32 s52, s52, 9472\n"
      "v_add_u32 v54, s52, v54\n"
      "v_lshlrev_b32 v55, 4, v1\n"
      "v_lshl_add_u32 v55, v3, 4, v55\n"
      "v_add_u32 v55, 7680, v55\n"
      "v_cmp_gt_u32 vcc, 4, v2\n"
      "s_nop 1\n"
      "v_cndmask_b32 v54, v55, v54, vcc\n"
      "v_lshlrev_b32 v55, 3, v1\n"
      "s_lshl_b32 s52, s40, 5\n"
      "s_add_u32 s52, s52, 4160\n"
      "v_add_u32 v60, s52, v55\n"
      "v_add_u32 v55, 7680, v55\n"
      "v_cmp_gt_u32 vcc, 4, v1\n"
      "s_nop 1\n"
      "v_cndmask_b32 v55, v55, v60, vcc\n"
      "v_lshlrev_b32 v60, 3, v1\n"
      "s_mul_i32 s52, s40, 528\n"
      "v_add_u32 v60, s52, v60\n"
      "s_lshl_b32 s52, s40, 3\n"
      "v_mov_b32 v61, s52\n"
      "v_lshlrev_b32 v62, 2, v1\n"
      "s_lshl_b32 s52, s44, 3\n"
      "s_add_u32 s52, s52, s42\n"
      "s_lshl_b32 s52, s52, 2\n"
      "s_add_u32 s52, s52, s40\n"
      "s_mul_i32 s62, s52, 384\n"
      "s_add_u32 s62, s62, 73728\n"
      "s_waitcnt vmcnt(43)\n"
      ".Lfd_a0j_5:\n"
      "v_lshrrev_b32 v11, 2, v1\n"
      "v_mul_u32_u24 v11, 3584, v11\n"
      "v_and_b32 v13, 3, v1\n"
      "v_lshl_add_u32 v11, v13, 4, v11\n"
      "s_mul_i32 s57, s40, 458752\n"
      "s_lshl_b32 s54, s46, 6\n"
      "s_add_u32 s57, s57, s54\n"
      "v_lshlrev_b32 v12, 13, v3\n"
      "v_lshl_add_u32 v12, v2, 4, v12\n"
      "s_lshl_b32 s59, s46, 15\n"
      "s_lshl_b32 s54, s40, 8\n"
      "s_add_u32 s59, s59, s54\n"
      "s_waitcnt lgkmcnt(0)\n"
      "s_barrier\n"
      "v_lshlrev_b32 v41, 4, v1\n"
      "ds_read_b128 v[16:19], v41 offset:6656\n"
      "s_waitcnt lgkmcnt(0)\n"
      "v_mfma_f32_16x16x32_bf16 v[32:35], a[0:3], v[16:19], 0\n"
      "v_mov_b32_dpp v20, v16 row_ror:12 row_mask:0xf bank_mask:0xf bound_ctrl:1\n"
      "v_mov_b32_dpp v21, v17 row_ror:12 row_mask:0xf bank_mask:0xf bound_ctrl:1\n"
      "v_mfma_f32_16x16x32_bf16 v[36:39], a[16:19], v[16:19], 0\n"
      "v_mov_b32_dpp v22, v18 row_ror:12 row_mask:0xf bank_mask:0xf bound_ctrl:1\n"
      "v_mov_b32_dpp v23, v19 row_ror:12 row_mask:0xf bank_mask:0xf bound_ctrl:1\n"
      "v_mov_b32_dpp v24, v16 row_ror:8 row_mask:0xf bank_mask:0xf bound_ctrl:1\n"
      "v_mov_b32_dpp v25, v17 row_ror:8 row_mask:0xf bank_mask:0xf bound_ctrl:1\n"
      "v_mfma_f32_16x16x32_bf16 v[32:35], a[4:7], v[20:23], v[32:35]\n"
      "v_mov_b32_dpp v26, v18 row_ror:8 row_mask:0xf bank_mask:0xf bound_ctrl:1\n"
      "v_mov_b32_dpp v27, v19 row_ror:8 row_mask:0xf bank_mask:0xf bound_ctrl:1\n"
      "v_mfma_f32_16x16x32_bf16 v[36:39], a[20:23], v[20:23], v[36:39]\n"
      "v_mov_b32_dpp v28, v16 row_ror:4 row_mask:0xf bank_mask:0xf bound_ctrl:1\n"
      "v_mov_b32_dpp v29, v17 row_ror:4 row_mask:0xf bank_mask:0xf bound_ctrl:1\n"
      "v_mfma_f32_16x16x32_bf16 v[32:35], a[8:11], v[24:27], v[32:35]\n"
      "v_mov_b32_dpp v30, v18 row_ror:4 row_mask:0xf bank_mask:0xf bound_ctrl:1\n"
      "v_mov_b32_dpp v31, v19 row_ror:4 row_mask:0xf bank_mask:0xf bound_ctrl:1\n"
      "v_mfma_f32_16x16x32_bf16 v[36:39], a[24:27], v[24:27], v[36:39]\n"
      "s_nop 0\n"
      "v_mfma_f32_16x16x32_bf16 v[32:35], a[12:15], v[28:31], v[32:35]\n"
      "v_mfma_f32_16x16x32_bf16 v[36:39], a[28:31], v[28:31], v[36:39]\n"
      "s_add_u32 s58, s57, 0\n"
      "buffer_load_dwordx4 a[172:175], v11, s[16:19], s58 offen\n"
      "s_add_u32 s58, s57, 57344\n"
      "buffer_load_dwordx4 a[176:179], v11, s[16:19], s58 offen\n"
      "s_nop 2\n"
      "v_max3_f32 v40, v32, v33, v34\n"
      "v_max3_f32 v40, v40, v35, v36\n"
      "v_max3_f32 v40, v40, v37, v38\n"
      "v_max_f32 v40, v40, v39\n"
      "v_mov_b32 v41, v40\n"
      "s_add_u32 s58, s57, 114688\n"
      "buffer_load_dwordx4 a[180:183], v11, s[16:19], s58 offen\n"
      "v_permlane16_swap_b32 v40, v41\n"
      "v_max_f32 v40, v40, v41\n"
      "v_mov_b32 v41, v40\n"
      "s_add_u32 s58, s57, 172032\n"
      "buffer_load_dwordx4 a[184:187], v11, s[16:19], s58 offen\n"
      "v_permlane32_swap_b32 v40, v41\n"
      "v_max_f32 v40, v40, v41\n"
      "v_mul_f32 v52, s63, v40\n"
      "v_fma_f32 v32, v32, s63, -v52\n"
      "v_fma_f32 v33, v33, s63, -v52\n"
      "v_fma_f32 v34, v34, s63, -v52\n"
      "v_fma_f32 v35, v35, s63, -v52\n"
      "s_add_u32 s58, s57, 229376\n"
      "buffer_load_dwordx4 a[188:191], v11, s[16:19], s58 offen\n"
      "v_fma_f32 v36, v36, s63, -v52\n"
      "v_fma_f32 v37, v37, s63, -v52\n"
      "v_fma_f32 v38, v38, s63, -v52\n"
      "v_fma_f32 v39, v39, s63, -v52\n"
      "s_add_u32 s58, s57, 286720\n"
      "buffer_load_dwordx4 a[192:195], v11, s[16:19], s58 offen\n"
      "v_exp_f32 v32, v32\n"
      "v_exp_f32 v33, v33\n"
      "v_exp_f32 v34, v34\n"
      "v_exp_f32 v35, v35\n"
      "s_add_u32 s58, s57, 344064\n"
      "buffer_load_dwordx4 a[196:199], v11, s[16:19], s58 offen\n"
      "v_exp_f32 v36, v36\n"
      "v_exp_f32 v37, v37\n"
      "v_exp_f32 v38, v38\n"
      "v_exp_f32 v39, v39\n"
      "s_add_u32 s58, s57, 401408\n"
      "buffer_load_dwordx4 a[200:203], v11, s[16:19], s58 offen\n"
      "v_cvt_pk_bf16_f32 v48, v32, v33\n"
      "v_cvt_pk_bf16_f32 v49, v34, v35\n"
      "v_cvt_pk_bf16_f32 v50, v36, v37\n"
      "v_cvt_pk_bf16_f32 v51, v38, v39\n"
      "s_add_u32 s58, s59, 0\n"
      "buffer_load_dwordx4 a[212:215], v12, s[20:23], s58 offen offset:0\n"
      "v_pk_add_f32 v[40:41], v[32:33], v[34:35]\n"
      "v_pk_add_f32 v[42:43], v[36:37], v[38:39]\n"
      "v_pk_add_f32 v[40:41], v[40:41], v[42:43]\n"
      "v_add_f32 v53, v40, v41\n"
      "s_cmp_eq_u32 s40, 0\n"
      "s_cbranch_scc0 .Lfd_a1w_8\n"
      "v_lshlrev_b32 v41, 5, v1\n"
      "s_mov_b32 s52, 4096\n"
      "buffer_load_dwordx4 a[204:207], v41, s[4:7], s52 offen sc1\n"
      "buffer_load_dwordx4 a[208:211], v41, s[4:7], s52 offen offset:16 sc1\n"
      "v_lshlrev_b32 v42, 13, v3\n"
      "v_lshl_add_u32 v42, v2, 3, v42\n"
      "s_lshl_b32 s53, s41, 19\n"
      "s_lshl_b32 s54, s40, 17\n"
      "s_add_u32 s53, s53, s54\n"
      "s_lshl_b32 s54, s42, 7\n"
      "s_add_u32 s53, s53, s54\n"
      "s_add_u32 s58, s53, 0\n"
      "buffer_load_dwordx2 v[64:65], v42, s[12:15], s58 offen offset:0\n"
      "buffer_load_dwordx2 v[66:67], v42, s[12:15], s58 offen offset:1024\n"
      "buffer_load_dwordx2 v[68:69], v42, s[12:15], s58 offen offset:2048\n"
      "buffer_load_dwordx2 v[70:71], v42, s[12:15], s58 offen offset:3072\n"
      "s_add_u32 s58, s53, 4096\n"
      "buffer_load_dwordx2 v[72:73], v42, s[12:15], s58 offen offset:0\n"
      "buffer_load_dwordx2 v[74:75], v42, s[12:15], s58 offen offset:1024\n"
      "buffer_load_dwordx2 v[76:77], v42, s[12:15], s58 offen offset:2048\n"
      "buffer_load_dwordx2 v[78:79], v42, s[12:15], s58 offen offset:3072\n"
      "s_add_u32 s58, s53, 32768\n"
      "buffer_load_dwordx2 v[80:81], v42, s[12:15], s58 offen offset:0\n"
      "buffer_load_dwordx2 v[82:83], v42, s[12:15], s58 offen offset:1024\n"
      "buffer_load_dwordx2 v[84:85], v42, s[12:15], s58 offen offset:2048\n"
      "buffer_load_dwordx2 v[86:87], v42, s[12:15], s58 offen offset:3072\n"
      "s_add_u32 s58, s53, 36864\n"
      "buffer_load_dwordx2 v[88:89], v42, s[12:15], s58 offen offset:0\n"
      "buffer_load_dwordx2 v[90:91], v42, s[12:15], s58 offen offset:1024\n"
      "buffer_load_dwordx2 v[92:93], v42, s[12:15], s58 offen offset:2048\n"
      "buffer_load_dwordx2 v[94:95], v42, s[12:15], s58 offen offset:3072\n"
      "s_add_u32 s58, s53, 65536\n"
      "buffer_load_dwordx2 v[96:97], v42, s[12:15], s58 offen offset:0\n"
      "buffer_load_dwordx2 v[98:99], v42, s[12:15], s58 offen offset:1024\n"
      "buffer_load_dwordx2 v[100:101], v42, s[12:15], s58 offen offset:2048\n"
      "buffer_load_dwordx2 v[102:103], v42, s[12:15], s58 offen offset:3072\n"
      "s_add_u32 s58, s53, 69632\n"
      "buffer_load_dwordx2 v[104:105], v42, s[12:15], s58 offen offset:0\n"
      "buffer_load_dwordx2 v[106:107], v42, s[12:15], s58 offen offset:1024\n"
      "buffer_load_dwordx2 v[108:109], v42, s[12:15], s58 offen offset:2048\n"
      "buffer_load_dwordx2 v[110:111], v42, s[12:15], s58 offen offset:3072\n"
      "s_add_u32 s58, s53, 98304\n"
      "buffer_load_dwordx2 v[112:113], v42, s[12:15], s58 offen offset:0\n"
      "buffer_load_dwordx2 v[114:115], v42, s[12:15], s58 offen offset:1024\n"
      "buffer_load_dwordx2 v[116:117], v42, s[12:15], s58 offen offset:2048\n"
      "buffer_load_dwordx2 v[118:119], v42, s[12:15], s58 offen offset:3072\n"
      "s_add_u32 s58, s53, 102400\n"
      "buffer_load_dwordx2 v[120:121], v42, s[12:15], s58 offen offset:0\n"
      "buffer_load_dwordx2 v[122:123], v42, s[12:15], s58 offen offset:1024\n"
      "buffer_load_dwordx2 v[124:125], v42, s[12:15], s58 offen offset:2048\n"
      "buffer_load_dwordx2 v[126:127], v42, s[12:15], s58 offen offset:3072\n"
      "s_branch .Lfd_a1j_9\n"
      ".Lfd_a1w_8:\n"
      "s_sub_u32 s52, s40, 1\n"
      "s_mul_i32 s53, s52, 3\n"
      "s_mul_i32 s52, s52, 11264\n"
      "v_lshlrev_b32 v40, 2, v3\n"
      "v_lshlrev_b32 v41, 8, v3\n"
      "v_add_u32 v41, s52, v41\n"
      "s_add_u32 s54, s53, 0\n"
      "s_and_b32 s54, s54, 3\n"
      "v_or_b32 v42, s54, v40\n"
      "v_xor_b32 v42, v42, v2\n"
      "v_lshl_add_u32 v44, v42, 4, v41\n"
      "s_add_u32 s54, s53, 1\n"
      "s_and_b32 s54, s54, 3\n"
      "v_or_b32 v42, s54, v40\n"
      "v_xor_b32 v42, v42, v2\n"
      "v_lshl_add_u32 v45, v42, 4, v41\n"
      "s_add_u32 s54, s53, 2\n"
      "s_and_b32 s54, s54, 3\n"
      "v_or_b32 v42, s54, v40\n"
      "v_xor_b32 v42, v42, v2\n"
      "v_lshl_add_u32 v46, v42, 4, v41\n"
      "s_add_u32 s54, s53, 3\n"
      "s_and_b32 s54, s54, 3\n"
      "v_or_b32 v42, s54, v40\n"
      "v_xor_b32 v42, v42, v2\n"
      "v_lshl_add_u32 v47, v42, 4, v41\n"
      "s_waitcnt vmcnt(41)\n"
      "ds_write_b128 v44, a[128:131] offset:18432\n"
      "ds_write_b128 v45, a[132:135] offset:19456\n"
      "ds_write_b128 v46, a[136:139] offset:20480\n"
      "ds_write_b128 v47, a[140:143] offset:21504\n"
      "ds_write_b128 v44, a[144:147] offset:22528\n"
      "ds_write_b128 v45, a[148:151] offset:23552\n"
      "ds_write_b128 v46, a[152:155] offset:24576\n"
      "ds_write_b128 v47, a[156:159] offset:25600\n"
      "ds_write_b128 v44, a[160:163] offset:26624\n"
      "ds_write_b128 v45, a[164:167] offset:27648\n"
      "ds_write_b128 v46, a[168:171] offset:28672\n"
      ".Lfd_a1j_9:\n"
      "v_lshrrev_b32 v40, 2, v2\n"
      "v_lshl_add_u32 v41, v3, 2, v40\n"
      "v_lshlrev_b32 v41, 8, v41\n"
      "v_lshl_or_b32 v42, v40, 2, v3\n"
      "v_bfe_u32 v43, v2, 1, 1\n"
      "v_xor_b32 v42, v42, v43\n"
      "v_lshl_add_u32 v41, v42, 4, v41\n"
      "v_and_b32 v43, 1, v2\n"
      "v_lshl_add_u32 v41, v43, 3, v41\n"
      "s_lshl_b32 s52, s40, 13\n"
      "v_add_u32 v41, s52, v41\n"
      "s_waitcnt lgkmcnt(0)\n"
      "s_barrier\n"
      "ds_read_b64_tr_b16 a[32:33], v41 offset:18432\n"
      "ds_read_b64_tr_b16 a[34:35], v41 offset:22528\n"
      "v_xor_b32 v42, 32, v41\n"
      "ds_read_b64_tr_b16 a[36:37], v42 offset:18432\n"
      "ds_read_b64_tr_b16 a[38:39], v42 offset:22528\n"
      "v_xor_b32 v42, 64, v41\n"
      "ds_read_b64_tr_b16 a[40:41], v42 offset:18432\n"
      "ds_read_b64_tr_b16 a[42:43], v42 offset:22528\n"
      "v_xor_b32 v42, 96, v41\n"
      "ds_read_b64_tr_b16 a[44:45], v42 offset:18432\n"
      "ds_read_b64_tr_b16 a[46:47], v42 offset:22528\n"
      "v_xor_b32 v42, 128, v41\n"
      "ds_read_b64_tr_b16 a[48:49], v42 offset:18432\n"
      "ds_read_b64_tr_b16 a[50:51], v42 offset:22528\n"
      "v_xor_b32 v42, 160, v41\n"
      "ds_read_b64_tr_b16 a[52:53], v42 offset:18432\n"
      "ds_read_b64_tr_b16 a[54:55], v42 offset:22528\n"
      "v_xor_b32 v42, 192, v41\n"
      "ds_read_b64_tr_b16 a[56:57], v42 offset:18432\n"
      "ds_read_b64_tr_b16 a[58:59], v42 offset:22528\n"
      "v_xor_b32 v42, 224, v41\n"
      "ds_read_b64_tr_b16 a[60:61], v42 offset:18432\n"
      "ds_read_b64_tr_b16 a[62:63], v42 offset:22528\n"
      "s_add_u32 s58, s59, 0\n"
      "buffer_load_dwordx4 a[216:219], v12, s[20:23], s58 offen offset:1024\n"
      "s_add_u32 s58, s59, 0\n"
      "buffer_load_dwordx4 a[220:223], v12, s[20:23], s58 offen offset:2048\n"
      "s_add_u32 s58, s59, 0\n"
      "buffer_load_dwordx4 a[224:227], v12, s[20:23], s58 offen offset:3072\n"
      "s_add_u32 s58, s59, 4096\n"
      "buffer_load_dwordx4 a[228:231], v12, s[20:23], s58 offen offset:0\n"
      "s_waitcnt lgkmcnt(0)\n"
      "v_mfma_f32_16x16x32_bf16 v[16:19], a[32:35], v[48:51], 0\n"
      "v_mov_b32 v58, v53\n"
      "s_nop 1\n"
      "v_permlane32_swap_b32 v53, v58\n"
      "v_mfma_f32_16x16x32_bf16 v[20:23], a[36:39], v[48:51], 0\n"
      "v_add_f32 v53, v53, v58\n"
      "v_mov_b32 v58, v53\n"
      "v_mfma_f32_16x16x32_bf16 v[24:27], a[40:43], v[48:51], 0\n"
      "s_nop 0\n"
      "v_permlane16_swap_b32 v53, v58\n"
      "v_add_f32 v53, v53, v58\n"
      "v_mfma_f32_16x16x32_bf16 v[28:31], a[44:47], v[48:51], 0\n"
      "ds_write_b64 v55, v[52:53]\n"
      "v_mfma_f32_16x16x32_bf16 v[32:35], a[48:51], v[48:51], 0\n"
      "ds_write_b128 v54, v[16:19] offset:0\n"
      "v_mfma_f32_16x16x32_bf16 v[36:39], a[52:55], v[48:51], 0\n"
      "ds_write_b128 v54, v[20:23] offset:64\n"
      "v_mfma_f32_16x16x32_bf16 v[40:43], a[56:59], v[48:51], 0\n"
      "ds_write_b128 v54, v[24:27] offset:128\n"
      "v_mfma_f32_16x16x32_bf16 v[44:47], a[60:63], v[48:51], 0\n"
      "ds_write_b128 v54, v[28:31] offset:192\n"
      "s_add_u32 s58, s59, 4096\n"
      "buffer_load_dwordx4 a[232:235], v12, s[20:23], s58 offen offset:1024\n"
      "s_add_u32 s58, s59, 4096\n"
      "buffer_load_dwordx4 a[236:239], v12, s[20:23], s58 offen offset:2048\n"
      "s_add_u32 s58, s59, 4096\n"
      "buffer_load_dwordx4 a[240:243], v12, s[20:23], s58 offen offset:3072\n"
      "ds_write_b128 v54, v[32:35] offset:256\n"
      "ds_write_b128 v54, v[36:39] offset:320\n"
      "ds_write_b128 v54, v[40:43] offset:384\n"
      "ds_write_b128 v54, v[44:47] offset:448\n"
      "s_cmp_lg_u32 s40, 0\n"
      "s_cbranch_scc1 .Lfd_fl_10\n"
      "s_waitcnt vmcnt(39)\n"
      "s_waitcnt lgkmcnt(0)\n"
      "s_mov_b32 s56, 131072\n"
      ".Lfd_poll_11:\n"
      "v_accvgpr_read_b32 v16, a204\n"
      "v_accvgpr_read_b32 v17, a205\n"
      "v_accvgpr_read_b32 v18, a206\n"
      "v_accvgpr_read_b32 v19, a207\n"
      "v_accvgpr_read_b32 v20, a208\n"
      "v_accvgpr_read_b32 v21, a209\n"
      "v_accvgpr_read_b32 v22, a210\n"
      "v_accvgpr_read_b32 v23, a211\n"
      "v_xor_b32 v16, s48, v16\n"
      "v_xor_b32 v17, s49, v17\n"
      "v_xor_b32 v18, s48, v18\n"
      "v_xor_b32 v19, s49, v19\n"
      "v_xor_b32 v20, s48, v20\n"
      "v_xor_b32 v21, s49, v21\n"
      "v_xor_b32 v22, s48, v22\n"
      "v_xor_b32 v23, s49, v23\n"
      "v_or3_b32 v16, v16, v17, v18\n"
      "v_or3_b32 v19, v19, v20, v21\n"
      "v_or3_b32 v16, v16, v22, v23\n"
      "v_or_b32 v16, v16, v19\n"
      "v_cmp_ne_u32 vcc, 0, v16\n"
      "s_cbranch_vccz .Lfd_done_12\n"
      "s_sub_u32 s56, s56, 1\n"
      "s_cbranch_scc1 .Lfd_done_12\n"
      "s_sleep 2\n"
      "v_lshlrev_b32 v41, 5, v1\n"
      "s_mov_b32 s52, 4096\n"
      "buffer_load_dwordx4 a[204:207], v41, s[4:7], s52 offen sc1\n"
      "buffer_load_dwordx4 a[208:211], v41, s[4:7], s52 offen offset:16 sc1\n"
      "s_waitcnt vmcnt(0)\n"
      "s_branch .Lfd_poll_11\n"
      ".Lfd_done_12:\n"
      "s_cmp_eq_u32 s3, 0\n"
      "s_cbranch_scc0 .Lfd_ep_13\n"
      "v_mov_b32 v16, s48\n"
      "v_mov_b32 v17, s49\n"
      "v_mov_b32 v18, 0\n"
      "s_mov_b64 exec, 1\n"
      "buffer_store_dwordx2 v[16:17], v18, s[4:7], 0 offen sc1\n"
      "s_mov_b64 exec, -1\n"
      ".Lfd_ep_13:\n"
      ".Lfd_fl_10:\n"
      "s_waitcnt lgkmcnt(0)\n"
      "s_barrier\n"
      "ds_read_b64 v[16:17], v61 offset:4160\n"
      "ds_read_b64 v[18:19], v61 offset:4192\n"
      "ds_read_b64 v[20:21], v61 offset:4224\n"
      "ds_read_b64 v[22:23], v61 offset:4256\n"
      "ds_read_b64 v[24:25], v60 offset:9472\n"
      "ds_read_b64 v[26:27], v60 offset:11584\n"
      "ds_read_b64 v[28:29], v60 offset:13696\n"
      "ds_read_b64 v[30:31], v60 offset:15808\n"
      "s_waitcnt lgkmcnt(4)\n"
      "v_max3_f32 v40, v16, v18, v20\n"
      "v_max_f32 v40, v40, v22\n"
      "v_sub_f32 v32, v16, v40\n"
      "v_sub_f32 v34, v18, v40\n"
      "v_sub_f32 v36, v20, v40\n"
      "v_sub_f32 v38, v22, v40\n"
      "v_exp_f32 v32, v32\n"
      "v_exp_f32 v34, v34\n"
      "v_exp_f32 v36, v36\n"
      "v_exp_f32 v38, v38\n"
      "s_waitcnt lgkmcnt(0)\n"
      "v_pk_mul_f32 v[42:43], v[32:33], v[24:25] op_sel_hi:[0,1]\n"
      "v_mul_f32 v41, v32, v17\n"
      "v_pk_fma_f32 v[42:43], v[34:35], v[26:27], v[42:43] op_sel_hi:[0,1,1]\n"
      "v_fmac_f32 v41, v34, v19\n"
      "v_pk_fma_f32 v[42:43], v[36:37], v[28:29], v[42:43] op_sel_hi:[0,1,1]\n"
      "v_fmac_f32 v41, v36, v21\n"
      "v_pk_fma_f32 v[42:43], v[38:39], v[30:31], v[42:43] op_sel_hi:[0,1,1]\n"
      "v_fmac_f32 v41, v38, v23\n"
      "v_cvt_pk_bf16_f32 v42, v42, v43\n"
      "v_min_u32 v42, -2, v42\n"
      "buffer_store_dword v42, v62, s[4:7], s62 offen sc1\n"
      "v_min_u32 v40, -2, v40\n"
      "v_min_u32 v41, -2, v41\n"
      "v_mov_b32 v44, 0\n"
      "s_mov_b64 exec, 1\n"
      "buffer_store_dwordx2 v[40:41], v44, s[4:7], s62 offen offset:256 sc1\n"
      "s_mov_b64 exec, -1\n"
      "v_and_b32 v56, 7, v2\n"
      "v_mul_u32_u24 v56, 1536, v56\n"
      "v_lshrrev_b32 v57, 3, v2\n"
      "v_lshl_add_u32 v56, v57, 6, v56\n"
      "v_lshl_add_u32 v56, v3, 4, v56\n"
      "v_and_b32 v57, 1, v3\n"
      "v_mul_u32_u24 v57, 6144, v57\n"
      "v_add_u32 v57, 256, v57\n"
      "s_mul_i32 s57, s44, 12288\n"
      "s_mul_i32 s52, s40, 384\n"
      "s_add_u32 s57, s57, s52\n"
      "s_add_u32 s57, s57, 73728\n"
      "s_add_u32 s59, s57, 3072\n"
      "s_nop 5\n"
      "s_nop 5\n"
      "s_nop 5\n"
      "s_nop 5\n"
      "s_nop 5\n"
      "s_nop 5\n"
      "s_nop 5\n"
      "s_nop 5\n"
      "s_nop 5\n"
      "s_nop 5\n"
      "s_nop 5\n"
      "s_nop 5\n"
      "s_nop 5\n"
      "s_nop 5\n"
      "s_nop 5\n"
      "s_nop 5\n"
      "s_nop 5\n"
      "s_nop 5\n"
      "s_nop 5\n"
      "s_nop 5\n"
      "s_cmp_eq_u32 s40, 0\n"
      "s_cbranch_scc1 .Lfd_wd_14\n"
      "s_nop 5\n"
      "s_nop 5\n"
      "s_nop 5\n"
      "s_nop 5\n"
      ".Lfd_wd_14:\n"
      "buffer_load_dwordx2 v[24:25], v57, s[4:7], s57 offen sc1\n"
      "buffer_load_dwordx2 v[26:27], v57, s[4:7], s57 offen offset:1536 sc1\n"
      "buffer_load_dwordx2 v[28:29], v57, s[4:7], s59 offen sc1\n"
      "buffer_load_dwordx2 v[30:31], v57, s[4:7], s59 offen offset:1536 sc1\n"
      "buffer_load_dwordx4 v[16:19], v56, s[4:7], s57 offen sc1\n"
      "buffer_load_dwordx4 v[20:23], v56, s[4:7], s57 offen offset:128 sc1\n"
      "v_lshlrev_b32 v11, 4, v2\n"
      "v_lshl_add_u32 v11, v3, 2, v11\n"
      "s_lshl_b32 s52, s40, 8\n"
      "v_add_u32 v11, s52, v11\n"
      "v_lshlrev_b32 v12, 2, v1\n"
      "v_lshlrev_b32 v13, 1, v1\n"
      "s_lshl_b32 s62, s44, 10\n"
      "s_lshl_b32 s53, s42, 7\n"
      "s_add_u32 s62, s62, s53\n"
      "s_add_u32 s62, s62, 860160\n"
      "s_waitcnt vmcnt(15)\n"
      "v_perm_b32 v192, v66, v64, s50\n"
      "v_perm_b32 v193, v70, v68, s50\n"
      "v_perm_b32 v194, v74, v72, s50\n"
      "v_perm_b32 v195, v78, v76, s50\n"
      "v_perm_b32 v196, v66, v64, s51\n"
      "v_perm_b32 v197, v70, v68, s51\n"
      "v_perm_b32 v198, v74, v72, s51\n"
      "v_perm_b32 v199, v78, v76, s51\n"
      "v_perm_b32 v200, v67, v65, s50\n"
      "v_perm_b32 v201, v71, v69, s50\n"
      "v_perm_b32 v202, v75, v73, s50\n"
      "v_perm_b32 v203, v79, v77, s50\n"
      "v_perm_b32 v204, v67, v65, s51\n"
      "v_perm_b32 v205, v71, v69, s51\n"
      "v_perm_b32 v206, v75, v73, s51\n"
      "v_perm_b32 v207, v79, v77, s51\n"
      "v_perm_b32 v208, v82, v80, s50\n"
      "v_perm_b32 v209, v86, v84, s50\n"
      "v_perm_b32 v210, v90, v88, s50\n"
      "v_perm_b32 v211, v94, v92, s50\n"
      "v_perm_b32 v212, v82, v80, s51\n"
      "v_perm_b32 v213, v86, v84, s51\n"
      "v_perm_b32 v214, v90, v88, s51\n"
      "v_perm_b32 v215, v94, v92, s51\n"
      "v_perm_b32 v216, v83, v81, s50\n"
      "v_perm_b32 v217, v87, v85, s50\n"
      "v_perm_b32 v218, v91, v89, s50\n"
      "v_perm_b32 v219, v95, v93, s50\n"
      "v_perm_b32 v220, v83, v81, s51\n"
      "v_perm_b32 v221, v87, v85, s51\n"
      "v_perm_b32 v222, v91, v89, s51\n"
      "v_perm_b32 v223, v95, v93, s51\n"
      "v_perm_b32 v224, v98, v96, s50\n"
      "v_perm_b32 v225, v102, v100, s50\n"
      "v_perm_b32 v226, v106, v104, s50\n"
      "v_perm_b32 v227, v110, v108, s50\n"
      "v_perm_b32 v228, v98, v96, s51\n"
      "v_perm_b32 v229, v102, v100, s51\n"
      "v_perm_b32 v230, v106, v104, s51\n"
      "v_perm_b32 v231, v110, v108, s51\n"
      "v_perm_b32 v232, v99, v97, s50\n"
      "v_perm_b32 v233, v103, v101, s50\n"
      "v_perm_b32 v234, v107, v105, s50\n"
      "v_perm_b32 v235, v111, v109, s50\n"
      "v_perm_b32 v236, v99, v97, s51\n"
      "v_perm_b32 v237, v103, v101, s51\n"
      "v_perm_b32 v238, v107, v105, s51\n"
      "v_perm_b32 v239, v111, v109, s51\n"
      "v_perm_b32 v240, v114, v112, s50\n"
      "v_perm_b32 v241, v118, v116, s50\n"
      "v_perm_b32 v242, v122, v120, s50\n"
      "v_perm_b32 v243, v126, v124, s50\n"
      "v_perm_b32 v244, v114, v112, s51\n"
      "v_perm_b32 v245, v118, v116, s51\n"
      "v_perm_b32 v246, v122, v120, s51\n"
      "v_perm_b32 v247, v126, v124, s51\n"
      "v_perm_b32 v248, v115, v113, s50\n"
      "v_perm_b32 v249, v119, v117, s50\n"
      "v_perm_b32 v250, v123, v121, s50\n"
      "v_perm_b32 v251, v127, v125, s50\n"
      "v_perm_b32 v252, v115, v113, s51\n"
      "v_perm_b32 v253, v119, v117, s51\n"
      "v_perm_b32 v254, v123, v121, s51\n"
      "v_perm_b32 v255, v127, v125, s51\n"
      "s_waitcnt vmcnt(0)\n"
      "v_max3_u32 v58, v24, v25, v26\n"
      "v_max3_u32 v58, v58, v27, v28\n"
      "v_max3_u32 v58, v58, v29, v30\n"
      "v_max3_u32 v58, v58, v31, v16\n"
      "v_max3_u32 v58, v58, v17, v18\n"
      "v_max3_u32 v58, v58, v19, v20\n"
      "v_max3_u32 v58, v58, v21, v22\n"
      "v_max_u32 v58, v58, v23\n"
      "v_cmp_eq_u32 vcc, -1, v58\n"
      "s_cbranch_vccz .Lfd_done_15\n"
      "s_mov_b32 s56, 131072\n"
      ".Lfd_poll_16:\n"
      "buffer_load_dwordx2 v[24:25], v57, s[4:7], s57 offen sc1\n"
      "buffer_load_dwordx2 v[26:27], v57, s[4:7], s57 offen offset:1536 sc1\n"
      "buffer_load_dwordx2 v[28:29], v57, s[4:7], s59 offen sc1\n"
      "buffer_load_dwordx2 v[30:31], v57, s[4:7], s59 offen offset:1536 sc1\n"
      "buffer_load_dwordx4 v[16:19], v56, s[4:7], s57 offen sc1\n"
      "buffer_load_dwordx4 v[20:23], v56, s[4:7], s57 offen offset:128 sc1\n"
      "s_waitcnt vmcnt(4)\n"
      "v_max3_u32 v58, v24, v25, v26\n"
      "s_waitcnt vmcnt(3)\n"
      "v_max3_u32 v58, v58, v27, v28\n"
      "s_waitcnt vmcnt(2)\n"
      "v_max3_u32 v58, v58, v29, v30\n"
      "s_waitcnt vmcnt(1)\n"
      "v_max3_u32 v58, v58, v31, v16\n"
      "v_max3_u32 v58, v58, v17, v18\n"
      "s_waitcnt vmcnt(0)\n"
      "v_max3_u32 v58, v58, v19, v20\n"
      "v_max3_u32 v58, v58, v21, v22\n"
      "v_max_u32 v58, v58, v23\n"
      "v_cmp_eq_u32 vcc, -1, v58\n"
      "s_cbranch_vccz .Lfd_done_17\n"
      "s_sub_u32 s56, s56, 1\n"
      "s_cbranch_scc1 .Lfd_done_17\n"
      "s_sleep 1\n"
      "s_branch .Lfd_poll_16\n"
      ".Lfd_done_17:\n"
      ".Lfd_done_15:\n"
      "v_mfma_f32_16x16x32_bf16 v[40:43], v[16:19], v[192:195], 0\n"
      "v_mov_b32_dpp v32, v16 row_ror:8 row_mask:0xf bank_mask:0xf bound_ctrl:1\n"
      "v_mov_b32_dpp v33, v17 row_ror:8 row_mask:0xf bank_mask:0xf bound_ctrl:1\n"
      "v_mfma_f32_16x16x32_bf16 v[44:47], v[16:19], v[196:199], 0\n"
      "v_mov_b32_dpp v34, v18 row_ror:8 row_mask:0xf bank_mask:0xf bound_ctrl:1\n"
      "v_mov_b32_dpp v35, v19 row_ror:8 row_mask:0xf bank_mask:0xf bound_ctrl:1\n"
      "v_mfma_f32_16x16x32_bf16 v[48:51], v[16:19], v[200:203], 0\n"
      "v_mov_b32_dpp v36, v20 row_ror:8 row_mask:0xf bank_mask:0xf bound_ctrl:1\n"
      "v_mov_b32_dpp v37, v21 row_ror:8 row_mask:0xf bank_mask:0xf bound_ctrl:1\n"
      "v_mfma_f32_16x16x32_bf16 v[52:55], v[16:19], v[204:207], 0\n"
      "v_mov_b32_dpp v38, v22 row_ror:8 row_mask:0xf bank_mask:0xf bound_ctrl:1\n"
      "v_mov_b32_dpp v39, v23 row_ror:8 row_mask:0xf bank_mask:0xf bound_ctrl:1\n"
      "v_mfma_f32_16x16x32_bf16 v[40:43], v[20:23], v[224:227], v[40:43]\n"
      "v_max3_f32 v58, v24, v26, v28\n"
      "v_max_f32 v58, v58, v30\n"
      "v_mfma_f32_16x16x32_bf16 v[44:47], v[20:23], v[228:231], v[44:47]\n"
      "v_mov_b32 v59, v58\n"
      "s_nop 1\n"
      "v_permlane16_swap_b32 v58, v59\n"
      "v_mfma_f32_16x16x32_bf16 v[48:51], v[20:23], v[232:235], v[48:51]\n"
      "v_max_f32 v58, v58, v59\n"
      "v_sub_f32 v60, v24, v58\n"
      "v_mfma_f32_16x16x32_bf16 v[52:55], v[20:23], v[236:239], v[52:55]\n"
      "v_sub_f32 v61, v26, v58\n"
      "v_sub_f32 v62, v28, v58\n"
      "v_mfma_f32_16x16x32_bf16 v[40:43], v[32:35], v[208:211], v[40:43]\n"
      "v_sub_f32 v63, v30, v58\n"
      "v_exp_f32 v60, v60\n"
      "v_mfma_f32_16x16x32_bf16 v[44:47], v[32:35], v[212:215], v[44:47]\n"
      "v_exp_f32 v61, v61\n"
      "v_exp_f32 v62, v62\n"
      "v_mfma_f32_16x16x32_bf16 v[48:51], v[32:35], v[216:219], v[48:51]\n"
      "v_exp_f32 v63, v63\n"
      "v_mul_f32 v58, v60, v25\n"
      "v_mfma_f32_16x16x32_bf16 v[52:55], v[32:35], v[220:223], v[52:55]\n"
      "v_fmac_f32 v58, v61, v27\n"
      "v_fmac_f32 v58, v62, v29\n"
      "v_mfma_f32_16x16x32_bf16 v[40:43], v[36:39], v[240:243], v[40:43]\n"
      "v_fmac_f32 v58, v63, v31\n"
      "v_mov_b32 v59, v58\n"
      "v_mfma_f32_16x16x32_bf16 v[44:47], v[36:39], v[244:247], v[44:47]\n"
      "s_nop 0\n"
      "v_permlane16_swap_b32 v58, v59\n"
      "v_add_f32 v58, v58, v59\n"
      "v_mfma_f32_16x16x32_bf16 v[48:51], v[36:39], v[248:251], v[48:51]\n"
      "v_rcp_f32 v58, v58\n"
      "v_mfma_f32_16x16x32_bf16 v[52:55], v[36:39], v[252:255], v[52:55]\n"
      "v_pk_mul_f32 v[60:61], v[60:61], v[58:59] op_sel_hi:[1,0]\n"
      "v_pk_mul_f32 v[62:63], v[62:63], v[58:59] op_sel_hi:[1,0]\n"
      "v_pk_mul_f32 v[24:25], v[60:61], v[40:41]\n"
      "v_pk_fma_f32 v[24:25], v[62:63], v[42:43], v[24:25]\n"
      "v_pk_mul_f32 v[26:27], v[60:61], v[44:45]\n"
      "v_pk_fma_f32 v[26:27], v[62:63], v[46:47], v[26:27]\n"
      "v_pk_mul_f32 v[28:29], v[60:61], v[48:49]\n"
      "v_pk_fma_f32 v[28:29], v[62:63], v[50:51], v[28:29]\n"
      "v_pk_mul_f32 v[30:31], v[60:61], v[52:53]\n"
      "v_pk_fma_f32 v[30:31], v[62:63], v[54:55], v[30:31]\n"
      "v_add_f32 v16, v24, v25\n"
      "v_add_f32 v17, v26, v27\n"
      "v_add_f32 v18, v28, v29\n"
      "v_add_f32 v19, v30, v31\n"
      "v_permlane16_swap_b32 v16, v17\n"
      "s_nop 0\n"
      "v_permlane16_swap_b32 v18, v19\n"
      "v_add_f32 v16, v16, v17\n"
      "v_add_f32 v18, v18, v19\n"
      "s_mov_b64 exec, s[94:95]\n"
      "ds_write2_b32 v11, v16, v18 offset0:0 offset1:2\n"
      "s_mov_b64 exec, -1\n"
      "s_waitcnt lgkmcnt(0)\n"
      "s_barrier\n"
      "s_cmp_lg_u32 s40, 0\n"
      "s_cbranch_scc1 .Lfd_of_18\n"
      "ds_read2st64_b32 v[16:17], v12 offset0:0 offset1:1\n"
      "ds_read2st64_b32 v[18:19], v12 offset0:2 offset1:3\n"
      "s_waitcnt lgkmcnt(0)\n"
      "v_pk_add_f32 v[16:17], v[16:17], v[18:19]\n"
      "v_add_f32 v16, v16, v17\n"
      "s_nop 1\n"
      "v_mov_b32_dpp v17, v16 quad_perm:[1,0,3,2] row_mask:0xf bank_mask:0xf bound_ctrl:1\n"
      "v_cvt_pk_bf16_f32 v16, v16, v17\n"
      "v_min_u32 v16, -2, v16\n"
      "s_mov_b64 exec, s[86:87]\n"
      "buffer_store_dword v16, v13, s[4:7], s62 offen sc1\n"
      "s_mov_b64 exec, -1\n"
      ".Lfd_of_18:\n"
      "v_lshlrev_b32 v56, 10, v3\n"
      "v_lshl_add_u32 v56, v2, 4, v56\n"
      "s_lshl_b32 s57, s43, 13\n"
      "s_lshl_b32 s52, s40, 8\n"
      "s_add_u32 s57, s57, s52\n"
      "s_add_u32 s57, s57, 860160\n"
      "s_add_u32 s59, s57, 4096\n"
      "v_lshrrev_b32 v58, 2, v1\n"
      "v_lshlrev_b32 v58, 6, v58\n"
      "v_and_b32 v59, 3, v1\n"
      "v_lshl_add_u32 v58, v59, 4, v58\n"
      "s_lshl_b32 s52, s40, 13\n"
      "s_add_u32 s52, s52, 51200\n"
      "v_add_u32 v58, s52, v58\n"
      "ds_write_b128 v58, a[172:175] offset:0\n"
      "ds_write_b128 v58, a[176:179] offset:1024\n"
      "ds_write_b128 v58, a[180:183] offset:2048\n"
      "ds_write_b128 v58, a[184:187] offset:3072\n"
      "ds_write_b128 v58, a[188:191] offset:4096\n"
      "ds_write_b128 v58, a[192:195] offset:5120\n"
      "ds_write_b128 v58, a[196:199] offset:6144\n"
      "ds_write_b128 v58, a[200:203] offset:7168\n"
      "v_accvgpr_read_b32 v64, a212\n"
      "v_accvgpr_read_b32 v65, a213\n"
      "v_accvgpr_read_b32 v66, a214\n"
      "v_accvgpr_read_b32 v67, a215\n"
      "v_accvgpr_read_b32 v68, a216\n"
      "v_accvgpr_read_b32 v69, a217\n"
      "v_accvgpr_read_b32 v70, a218\n"
      "v_accvgpr_read_b32 v71, a219\n"
      "v_accvgpr_read_b32 v72, a220\n"
      "v_accvgpr_read_b32 v73, a221\n"
      "v_accvgpr_read_b32 v74, a222\n"
      "v_accvgpr_read_b32 v75, a223\n"
      "v_accvgpr_read_b32 v76, a224\n"
      "v_accvgpr_read_b32 v77, a225\n"
      "v_accvgpr_read_b32 v78, a226\n"
      "v_accvgpr_read_b32 v79, a227\n"
      "v_accvgpr_read_b32 v80, a228\n"
      "v_accvgpr_read_b32 v81, a229\n"
      "v_accvgpr_read_b32 v82, a230\n"
      "v_accvgpr_read_b32 v83, a231\n"
      "v_accvgpr_read_b32 v84, a232\n"
      "v_accvgpr_read_b32 v85, a233\n"
      "v_accvgpr_read_b32 v86, a234\n"
      "v_accvgpr_read_b32 v87, a235\n"
      "v_accvgpr_read_b32 v88, a236\n"
      "v_accvgpr_read_b32 v89, a237\n"
      "v_accvgpr_read_b32 v90, a238\n"
      "v_accvgpr_read_b32 v91, a239\n"
      "v_accvgpr_read_b32 v92, a240\n"
      "v_accvgpr_read_b32 v93, a241\n"
      "v_accvgpr_read_b32 v94, a242\n"
      "v_accvgpr_read_b32 v95, a243\n"
      "s_nop 5\n"
      "s_nop 5\n"
      "s_nop 5\n"
      "s_nop 5\n"
      "s_nop 5\n"
      "s_nop 5\n"
      "s_nop 5\n"
      "s_nop 5\n"
      "s_nop 5\n"
      "s_nop 5\n"
      "s_nop 5\n"
      "s_nop 5\n"
      "s_nop 5\n"
      "s_nop 5\n"
      "s_nop 5\n"
      "s_nop 5\n"
      "s_cmp_eq_u32 s40, 0\n"
      "s_cbranch_scc1 .Lfd_wd_19\n"
      "s_nop 5\n"
      "s_nop 5\n"
      "s_nop 5\n"
      "s_nop 5\n"
      ".Lfd_wd_19:\n"
      "buffer_load_dwordx4 v[16:19], v56, s[4:7], s57 offen sc1\n"
      "buffer_load_dwordx4 v[20:23], v56, s[4:7], s59 offen sc1\n"
      "v_perm_b32 v96, v68, v64, s50\n"
      "v_perm_b32 v97, v76, v72, s50\n"
      "v_perm_b32 v98, v84, v80, s50\n"
      "v_perm_b32 v99, v92, v88, s50\n"
      "v_perm_b32 v100, v68, v64, s51\n"
      "v_perm_b32 v101, v76, v72, s51\n"
      "v_perm_b32 v102, v84, v80, s51\n"
      "v_perm_b32 v103, v92, v88, s51\n"
      "v_perm_b32 v104, v69, v65, s50\n"
      "v_perm_b32 v105, v77, v73, s50\n"
      "v_perm_b32 v106, v85, v81, s50\n"
      "v_perm_b32 v107, v93, v89, s50\n"
      "v_perm_b32 v108, v69, v65, s51\n"
      "v_perm_b32 v109, v77, v73, s51\n"
      "v_perm_b32 v110, v85, v81, s51\n"
      "v_perm_b32 v111, v93, v89, s51\n"
      "v_perm_b32 v112, v70, v66, s50\n"
      "v_perm_b32 v113, v78, v74, s50\n"
      "v_perm_b32 v114, v86, v82, s50\n"
      "v_perm_b32 v115, v94, v90, s50\n"
      "v_perm_b32 v116, v70, v66, s51\n"
      "v_perm_b32 v117, v78, v74, s51\n"
      "v_perm_b32 v118, v86, v82, s51\n"
      "v_perm_b32 v119, v94, v90, s51\n"
      "v_perm_b32 v120, v71, v67, s50\n"
      "v_perm_b32 v121, v79, v75, s50\n"
      "v_perm_b32 v122, v87, v83, s50\n"
      "v_perm_b32 v123, v95, v91, s50\n"
      "v_perm_b32 v124, v71, v67, s51\n"
      "v_perm_b32 v125, v79, v75, s51\n"
      "v_perm_b32 v126, v87, v83, s51\n"
      "v_perm_b32 v127, v95, v91, s51\n"
      "s_waitcnt lgkmcnt(0)\n"
      "s_barrier\n"
      "v_bfe_u32 v58, v2, 1, 1\n"
      "v_lshlrev_b32 v58, 5, v58\n"
      "v_lshl_add_u32 v58, v3, 3, v58\n"
      "v_lshrrev_b32 v59, 2, v2\n"
      "v_add_u32 v58, v58, v59\n"
      "v_lshlrev_b32 v58, 6, v58\n"
      "v_and_b32 v59, 1, v2\n"
      "v_lshl_add_u32 v58, v59, 3, v58\n"
      "s_lshl_b32 s52, s40, 4\n"
      "s_add_u32 s52, s52, 51200\n"
      "v_add_u32 v58, s52, v58\n"
      "ds_read_b64_tr_b16 a[64:65], v58 offset:0\n"
      "ds_read_b64_tr_b16 a[66:67], v58 offset:256\n"
      "ds_read_b64_tr_b16 a[68:69], v58 offset:4096\n"
      "ds_read_b64_tr_b16 a[70:71], v58 offset:4352\n"
      "ds_read_b64_tr_b16 a[72:73], v58 offset:8192\n"
      "ds_read_b64_tr_b16 a[74:75], v58 offset:8448\n"
      "ds_read_b64_tr_b16 a[76:77], v58 offset:12288\n"
      "ds_read_b64_tr_b16 a[78:79], v58 offset:12544\n"
      "ds_read_b64_tr_b16 a[80:81], v58 offset:16384\n"
      "ds_read_b64_tr_b16 a[82:83], v58 offset:16640\n"
      "ds_read_b64_tr_b16 a[84:85], v58 offset:20480\n"
      "ds_read_b64_tr_b16 a[86:87], v58 offset:20736\n"
      "ds_read_b64_tr_b16 a[88:89], v58 offset:24576\n"
      "ds_read_b64_tr_b16 a[90:91], v58 offset:24832\n"
      "ds_read_b64_tr_b16 a[92:93], v58 offset:28672\n"
      "ds_read_b64_tr_b16 a[94:95], v58 offset:28928\n"
      "v_mov_b32 v32, 0x3f803f80\n"
      "v_mov_b32 v33, 0x3f803f80\n"
      "v_mov_b32 v34, 0x3f803f80\n"
      "v_mov_b32 v35, 0x3f803f80\n"
      "s_lshl_b32 s52, s40, 8\n"
      "v_and_b32 v57, 1, v3\n"
      "v_lshlrev_b32 v57, 3, v57\n"
      "v_lshl_add_u32 v57, v2, 4, v57\n"
      "v_add_u32 v57, s52, v57\n"
      "ds_read_b64 v[28:29], v57 offset:5632\n"
      "v_lshrrev_b32 v11, 5, v1\n"
      "v_lshlrev_b32 v11, 2, v11\n"
      "s_lshl_b32 s53, s40, 3\n"
      "v_add_u32 v11, s53, v11\n"
      "v_and_b32 v12, 1, v2\n"
      "v_lshlrev_b32 v12, 6, v12\n"
      "v_lshl_add_u32 v12, v3, 4, v12\n"
      "v_lshlrev_b32 v13, 1, v1\n"
      "s_lshl_b32 s53, s40, 4\n"
      "v_add_u32 v13, s53, v13\n"
      "v_lshlrev_b32 v14, 4, v3\n"
      "v_lshrrev_b32 v15, 3, v2\n"
      "v_mul_u32_u24 v15, 7168, v15\n"
      "v_and_b32 v58, 7, v2\n"
      "v_lshl_add_u32 v15, v58, 4, v15\n"
      "s_lshl_b32 s52, s43, 3\n"
      "s_lshl_b32 s53, s40, 1\n"
      "s_add_u32 s52, s52, s53\n"
      "s_mul_i32 s52, s52, 56\n"
      "s_add_u32 s52, s52, s45\n"
      "s_lshl_b32 s62, s52, 7\n"
      "s_add_u32 s62, s62, 925696\n"
      "s_waitcnt lgkmcnt(0)\n"
      "v_mfma_f32_16x16x32_bf16 v[36:39], v[32:35], a[64:67], 0\n"
      "v_mfma_f32_16x16x32_bf16 v[36:39], v[32:35], a[68:71], v[36:39]\n"
      "v_mfma_f32_16x16x32_bf16 v[36:39], v[32:35], a[72:75], v[36:39]\n"
      "v_mfma_f32_16x16x32_bf16 v[36:39], v[32:35], a[76:79], v[36:39]\n"
      "v_mfma_f32_16x16x32_bf16 v[36:39], v[32:35], a[80:83], v[36:39]\n"
      "v_mfma_f32_16x16x32_bf16 v[36:39], v[32:35], a[84:87], v[36:39]\n"
      "v_mfma_f32_16x16x32_bf16 v[36:39], v[32:35], a[88:91], v[36:39]\n"
      "v_mfma_f32_16x16x32_bf16 v[36:39], v[32:35], a[92:95], v[36:39]\n"
      "s_nop 7\n"
      "v_add_f32_dpp v5, v36, v36 row_ror:8 row_mask:0xf bank_mask:0xf bound_ctrl:1\n"
      "s_waitcnt vmcnt(0)\n"
      "v_max3_u32 v58, v16, v17, v18\n"
      "v_max3_u32 v58, v58, v19, v20\n"
      "v_max3_u32 v58, v58, v21, v22\n"
      "v_max_u32 v58, v58, v23\n"
      "v_cmp_eq_u32 vcc, -1, v58\n"
      "s_cbranch_vccz .Lfd_done_20\n"
      "s_mov_b32 s56, 131072\n"
      ".Lfd_poll_21:\n"
      "buffer_load_dwordx4 v[16:19], v56, s[4:7], s57 offen sc1\n"
      "buffer_load_dwordx4 v[20:23], v56, s[4:7], s59 offen sc1\n"
      "s_waitcnt vmcnt(1)\n"
      "v_max3_u32 v58, v16, v17, v18\n"
      "s_waitcnt vmcnt(0)\n"
      "v_max3_u32 v58, v58, v19, v20\n"
      "v_max3_u32 v58, v58, v21, v22\n"
      "v_max_u32 v58, v58, v23\n"
      "v_cmp_eq_u32 vcc, -1, v58\n"
      "s_cbranch_vccz .Lfd_done_22\n"
      "s_sub_u32 s56, s56, 1\n"
      "s_cbranch_scc1 .Lfd_done_22\n"
      "s_sleep 1\n"
      "s_branch .Lfd_poll_21\n"
      ".Lfd_done_22:\n"
      ".Lfd_done_20:\n"
      "v_mfma_f32_16x16x32_bf16 v[24:27], v[6:9], v[16:19], 0\n"
      "v_mfma_f32_16x16x32_bf16 v[24:27], v[6:9], v[20:23], v[24:27]\n"
      "s_waitcnt lgkmcnt(0)\n"
      "v_lshlrev_b32 v32, 16, v28\n"
      "v_and_b32 v33, 0xffff0000, v28\n"
      "v_lshlrev_b32 v34, 16, v29\n"
      "v_and_b32 v35, 0xffff0000, v29\n"
      "s_nop 2\n"
      "v_cvt_pk_bf16_f32 v30, v24, v25\n"
      "v_cvt_pk_bf16_f32 v31, v26, v27\n"
      "v_lshlrev_b32 v36, 16, v30\n"
      "v_and_b32 v37, 0xffff0000, v30\n"
      "v_lshlrev_b32 v38, 16, v31\n"
      "v_and_b32 v39, 0xffff0000, v31\n"
      "v_pk_add_f32 v[32:33], v[32:33], v[36:37]\n"
      "v_pk_add_f32 v[34:35], v[34:35], v[38:39]\n"
      "v_cvt_pk_bf16_f32 v30, v32, v33\n"
      "v_cvt_pk_bf16_f32 v31, v34, v35\n"
      "ds_write_b64 v57, v[30:31] offset:4608\n"
      "v_lshlrev_b32 v32, 16, v30\n"
      "v_and_b32 v33, 0xffff0000, v30\n"
      "v_lshlrev_b32 v34, 16, v31\n"
      "v_and_b32 v35, 0xffff0000, v31\n"
      "v_pk_add_f32 v[36:37], v[32:33], v[34:35]\n"
      "v_pk_mul_f32 v[38:39], v[32:33], v[32:33]\n"
      "v_pk_fma_f32 v[38:39], v[34:35], v[34:35], v[38:39]\n"
      "v_add_f32 v36, v36, v37\n"
      "v_add_f32 v37, v38, v39\n"
      "s_nop 1\n"
      "v_permlane32_swap_b32 v36, v37\n"
      "v_add_f32 v36, v36, v37\n"
      "v_mov_b32 v37, v36\n"
      "s_nop 1\n"
      "v_permlane16_swap_b32 v36, v37\n"
      "v_add_f32 v36, v36, v37\n"
      "s_nop 1\n"
      "v_add_f32_dpp v36, v36, v36 quad_perm:[1,0,3,2] row_mask:0xf bank_mask:0xf bound_ctrl:1\n"
      "s_nop 1\n"
      "v_add_f32_dpp v36, v36, v36 quad_perm:[2,3,0,1] row_mask:0xf bank_mask:0xf bound_ctrl:1\n"
      "s_nop 1\n"
      "v_add_f32_dpp v36, v36, v36 row_ror:4 row_mask:0xf bank_mask:0xf bound_ctrl:1\n"
      "s_nop 1\n"
      "v_add_f32_dpp v36, v36, v36 row_ror:8 row_mask:0xf bank_mask:0xf bound_ctrl:1\n"
      "s_mov_b64 exec, s[92:93]\n"
      "ds_write_b32 v11, v36 offset:4096\n"
      "s_mov_b64 exec, -1\n"
      "v_mov_b32 v59, 0\n"
      "s_waitcnt lgkmcnt(0)\n"
      "s_barrier\n"
      "s_cmp_ge_u32 s45, 56\n"
      "s_cbranch_scc1 .Lfd_f1_23\n"
      "ds_read_b128 v[16:19], v12 offset:4608\n"
      "ds_read_b128 v[20:23], v12 offset:4736\n"
      "ds_read_b128 v[24:27], v12 offset:4864\n"
      "ds_read_b128 v[28:31], v12 offset:4992\n"
      "ds_read_b128 v[32:35], v12 offset:5120\n"
      "ds_read_b128 v[36:39], v12 offset:5248\n"
      "ds_read_b128 v[40:43], v12 offset:5376\n"
      "ds_read_b128 v[44:47], v12 offset:5504\n"
      "ds_read_b128 v[48:51], v59 offset:4096\n"
      "ds_read_b128 v[52:55], v59 offset:4112\n"
      "s_waitcnt lgkmcnt(9)\n"
      "v_mfma_f32_16x16x32_bf16 v[60:63], v[16:19], a[64:67], 0\n"
      "s_waitcnt lgkmcnt(8)\n"
      "v_mfma_f32_16x16x32_bf16 v[60:63], v[20:23], a[68:71], v[60:63]\n"
      "s_waitcnt lgkmcnt(7)\n"
      "v_mfma_f32_16x16x32_bf16 v[60:63], v[24:27], a[72:75], v[60:63]\n"
      "s_waitcnt lgkmcnt(6)\n"
      "v_mfma_f32_16x16x32_bf16 v[60:63], v[28:31], a[76:79], v[60:63]\n"
      "s_waitcnt lgkmcnt(5)\n"
      "v_mfma_f32_16x16x32_bf16 v[60:63], v[32:35], a[80:83], v[60:63]\n"
      "s_waitcnt lgkmcnt(4)\n"
      "v_mfma_f32_16x16x32_bf16 v[60:63], v[36:39], a[84:87], v[60:63]\n"
      "s_waitcnt lgkmcnt(0)\n"
      "v_pk_add_f32 v[48:49], v[48:49], v[50:51]\n"
      "v_pk_add_f32 v[52:53], v[52:53], v[54:55]\n"
      "v_pk_add_f32 v[48:49], v[48:49], v[52:53]\n"
      "s_waitcnt lgkmcnt(3)\n"
      "v_mfma_f32_16x16x32_bf16 v[60:63], v[40:43], a[88:91], v[60:63]\n"
      "v_mul_f32 v48, 0x3a800000, v48\n"
      "v_mul_f32 v49, 0x3a800000, v49\n"
      "v_fma_f32 v49, -v48, v48, v49\n"
      "s_waitcnt lgkmcnt(2)\n"
      "v_mfma_f32_16x16x32_bf16 v[60:63], v[44:47], a[92:95], v[60:63]\n"
      "v_max_f32 v49, 0, v49\n"
      "v_add_f32 v49, 0x3727c5ac, v49\n"
      "v_rsq_f32 v49, v49\n"
      "s_nop 4\n"
      "v_cndmask_b32 v16, v61, v60, s[96:97]\n"
      "s_nop 1\n"
      "v_add_f32_dpp v16, v16, v16 row_ror:8 row_mask:0xf bank_mask:0xf bound_ctrl:1\n"
      "v_fma_f32 v16, -v48, v5, v16\n"
      "v_mul_f32 v16, v16, v49\n"
      "v_cvt_pk_bf16_f32 v16, v16, v16\n"
      "v_lshlrev_b32 v16, 16, v16\n"
      "v_mul_f32 v17, 0xbfb8aa3b, v16\n"
      "v_exp_f32 v17, v17\n"
      "s_nop 0\n"
      "v_add_f32 v17, 1.0, v17\n"
      "v_rcp_f32 v17, v17\n"
      "s_nop 0\n"
      "v_mul_f32 v16, v16, v17\n"
      "s_nop 1\n"
      "v_mov_b32_dpp v17, v16 quad_perm:[1,0,3,2] row_mask:0xf bank_mask:0xf bound_ctrl:1\n"
      "v_cvt_pk_bf16_f32 v16, v16, v17\n"
      "s_mov_b64 exec, 0x55\n"
      "ds_write_b32 v13, v16 offset:4288\n"
      "s_mov_b64 exec, -1\n"
      ".Lfd_f1_23:\n"
      "s_waitcnt lgkmcnt(0)\n"
      "s_barrier\n"
      "s_cmp_ge_u32 s45, 56\n"
      "s_cbranch_scc1 .Lfd_f2_24\n"
      "ds_read_b128 v[16:19], v14 offset:4288\n"
      "s_waitcnt lgkmcnt(0)\n"
      "v_mfma_f32_16x16x32_bf16 v[20:23], v[16:19], v[96:99], 0\n"
      "v_mfma_f32_16x16x32_bf16 v[24:27], v[16:19], v[100:103], 0\n"
      "v_mfma_f32_16x16x32_bf16 v[28:31], v[16:19], v[104:107], 0\n"
      "v_mfma_f32_16x16x32_bf16 v[32:35], v[16:19], v[108:111], 0\n"
      "v_mfma_f32_16x16x32_bf16 v[36:39], v[16:19], v[112:115], 0\n"
      "v_mfma_f32_16x16x32_bf16 v[40:43], v[16:19], v[116:119], 0\n"
      "v_mfma_f32_16x16x32_bf16 v[44:47], v[16:19], v[120:123], 0\n"
      "v_mfma_f32_16x16x32_bf16 v[48:51], v[16:19], v[124:127], 0\n"
      "s_nop 1\n"
      "v_cvt_pk_bf16_f32 v52, v20, v24\n"
      "v_min_u32 v52, -2, v52\n"
      "v_cvt_pk_bf16_f32 v53, v28, v32\n"
      "v_min_u32 v53, -2, v53\n"
      "v_cvt_pk_bf16_f32 v54, v36, v40\n"
      "v_min_u32 v54, -2, v54\n"
      "v_cvt_pk_bf16_f32 v55, v44, v48\n"
      "v_min_u32 v55, -2, v55\n"
      "s_mov_b64 exec, 0xffff\n"
      "buffer_store_dwordx4 v[52:55], v15, s[4:7], s62 offen sc1\n"
      "s_mov_b64 exec, -1\n"
      ".Lfd_f2_24:\n"
      "v_mov_b32 v16, 0x3f803f80\n"
      "v_mov_b32 v17, 0x3f803f80\n"
      "v_mov_b32 v18, 0x3f803f80\n"
      "v_mov_b32 v19, 0x3f803f80\n"
      "v_and_b32 v60, 7, v2\n"
      "v_lshlrev_b32 v60, 5, v60\n"
      "v_lshl_add_u32 v60, v3, 8, v60\n"
      "s_lshl_b32 s52, s40, 9\n"
      "s_add_u32 s52, s52, 2048\n"
      "v_add_u32 v60, s52, v60\n"
      "v_mfma_f32_16x16x32_bf16 v[24:27], v[16:19], v[128:131], 0\n"
      "v_mfma_f32_16x16x32_bf16 v[28:31], v[16:19], v[132:135], 0\n"
      "v_mfma_f32_16x16x32_bf16 v[32:35], v[16:19], v[136:139], 0\n"
      "v_mfma_f32_16x16x32_bf16 v[36:39], v[16:19], v[140:143], 0\n"
      "v_mfma_f32_16x16x32_bf16 v[40:43], v[16:19], v[144:147], 0\n"
      "v_mfma_f32_16x16x32_bf16 v[44:47], v[16:19], v[148:151], 0\n"
      "v_mfma_f32_16x16x32_bf16 v[48:51], v[16:19], v[152:155], 0\n"
      "v_mfma_f32_16x16x32_bf16 v[52:55], v[16:19], v[156:159], 0\n"
      "v_mfma_f32_16x16x32_bf16 v[24:27], v[16:19], v[160:163], v[24:27]\n"
      "v_mfma_f32_16x16x32_bf16 v[28:31], v[16:19], v[164:167], v[28:31]\n"
      "v_mfma_f32_16x16x32_bf16 v[32:35], v[16:19], v[168:171], v[32:35]\n"
      "v_mfma_f32_16x16x32_bf16 v[36:39], v[16:19], v[172:175], v[36:39]\n"
      "v_mfma_f32_16x16x32_bf16 v[40:43], v[16:19], v[176:179], v[40:43]\n"
      "v_mfma_f32_16x16x32_bf16 v[44:47], v[16:19], v[180:183], v[44:47]\n"
      "v_mfma_f32_16x16x32_bf16 v[48:51], v[16:19], v[184:187], v[48:51]\n"
      "v_mfma_f32_16x16x32_bf16 v[52:55], v[16:19], v[188:191], v[52:55]\n"
      "s_mov_b64 exec, s[88:89]\n"
      "s_nop 0\n"
      "ds_write2_b32 v60, v24, v28 offset0:0 offset1:1\n"
      "s_nop 0\n"
      "ds_write2_b32 v60, v32, v36 offset0:2 offset1:3\n"
      "s_nop 0\n"
      "ds_write2_b32 v60, v40, v44 offset0:4 offset1:5\n"
      "s_nop 0\n"
      "ds_write2_b32 v60, v48, v52 offset0:6 offset1:7\n"
      "s_mov_b64 exec, -1\n"
      "v_lshlrev_b32 v61, 2, v1\n"
      "s_waitcnt lgkmcnt(0)\n"
      "s_barrier\n"
      "ds_read2st64_b32 v[16:17], v61 offset0:8 offset1:9\n"
      "ds_read2st64_b32 v[18:19], v61 offset0:10 offset1:11\n"
      "ds_read2st64_b32 v[20:21], v61 offset0:12 offset1:13\n"
      "ds_read2st64_b32 v[22:23], v61 offset0:14 offset1:15\n"
      "s_waitcnt lgkmcnt(0)\n"
      "v_pk_add_f32 v[16:17], v[16:17], v[18:19]\n"
      "v_pk_add_f32 v[20:21], v[20:21], v[22:23]\n"
      "v_pk_add_f32 v[16:17], v[16:17], v[20:21]\n"
      "v_add_f32 v4, v16, v17\n"
      "s_cmp_eq_u32 s47, 1\n"
      "s_cbranch_scc0 .Lfd_rend_25\n"
      "v_lshrrev_b32 v56, 3, v2\n"
      "v_lshl_add_u32 v56, v56, 2, v3\n"
      "v_lshlrev_b32 v56, 7, v56\n"
      "v_and_b32 v57, 7, v2\n"
      "v_lshl_add_u32 v56, v57, 4, v56\n"
      "s_lshl_b32 s52, s43, 3\n"
      "s_add_u32 s52, s52, s42\n"
      "s_mul_i32 s52, s52, 56\n"
      "s_lshl_b32 s53, s40, 4\n"
      "s_add_u32 s52, s52, s53\n"
      "s_lshl_b32 s57, s52, 7\n"
      "s_add_u32 s57, s57, 925696\n"
      "s_add_u32 s59, s57, 1024\n"
      "s_cmp_eq_u32 s40, 3\n"
      "s_cselect_b32 s59, 1449984, s59\n"
      "v_lshlrev_b32 v57, 5, v2\n"
      "v_lshl_add_u32 v57, v3, 4, v57\n"
      "s_lshl_b32 s52, s40, 8\n"
      "v_add_u32 v57, s52, v57\n"
      "v_lshlrev_b32 v61, 2, v1\n"
      "v_lshlrev_b32 v62, 1, v1\n"
      "s_lshl_b32 s53, s42, 7\n"
      "v_add_u32 v63, s53, v62\n"
      "s_lshl_b32 s62, s43, 10\n"
      "s_add_u32 s62, s62, s53\n"
      "s_add_u32 s62, s62, 1384448\n"
      "ds_read_u16 v28, v63 offset:4608\n"
      "s_mov_b32 s56, 131072\n"
      ".Lfd_poll_26:\n"
      "buffer_load_dwordx4 v[16:19], v56, s[4:7], s57 offen sc1\n"
      "buffer_load_dwordx4 v[20:23], v56, s[4:7], s59 offen sc1\n"
      "s_waitcnt vmcnt(1)\n"
      "v_max3_u32 v58, v16, v17, v18\n"
      "s_waitcnt vmcnt(0)\n"
      "v_max3_u32 v58, v58, v19, v20\n"
      "v_max3_u32 v58, v58, v21, v22\n"
      "v_max_u32 v58, v58, v23\n"
      "v_cmp_eq_u32 vcc, -1, v58\n"
      "s_cbranch_vccz .Lfd_done_27\n"
      "s_sub_u32 s56, s56, 1\n"
      "s_cbranch_scc1 .Lfd_done_27\n"
      "s_sleep 1\n"
      "s_branch .Lfd_poll_26\n"
      ".Lfd_done_27:\n"
      "v_mfma_f32_16x16x32_bf16 v[24:27], v[6:9], v[16:19], 0\n"
      "v_mfma_f32_16x16x32_bf16 v[24:27], v[6:9], v[20:23], v[24:27]\n"
      "s_waitcnt lgkmcnt(0)\n"
      "v_lshlrev_b32 v28, 16, v28\n"
      "s_nop 5\n"
      "v_add_f32_dpp v24, v24, v24 row_ror:8 row_mask:0xf bank_mask:0xf bound_ctrl:1\n"
      "v_add_f32_dpp v25, v25, v25 row_ror:8 row_mask:0xf bank_mask:0xf bound_ctrl:1\n"
      "v_add_f32_dpp v26, v26, v26 row_ror:8 row_mask:0xf bank_mask:0xf bound_ctrl:1\n"
      "v_add_f32_dpp v27, v27, v27 row_ror:8 row_mask:0xf bank_mask:0xf bound_ctrl:1\n"
      "s_mov_b64 exec, 0x00ff00ff\n"
      "ds_write_b128 v57, v[24:27] offset:0\n"
      "s_mov_b64 exec, -1\n"
      "s_waitcnt lgkmcnt(0)\n"
      "s_barrier\n"
      "s_cmp_lg_u32 s40, 0\n"
      "s_cbranch_scc1 .Lfd_rf_28\n"
      "ds_read2st64_b32 v[16:17], v61 offset0:0 offset1:1\n"
      "ds_read2st64_b32 v[18:19], v61 offset0:2 offset1:3\n"
      "s_waitcnt lgkmcnt(0)\n"
      "v_pk_add_f32 v[16:17], v[16:17], v[18:19]\n"
      "v_add_f32 v16, v16, v17\n"
      "v_cvt_pk_bf16_f32 v16, v16, v16\n"
      "v_lshlrev_b32 v16, 16, v16\n"
      "v_add_f32 v16, v16, v28\n"
      "s_nop 1\n"
      "v_mov_b32_dpp v17, v16 quad_perm:[1,0,3,2] row_mask:0xf bank_mask:0xf bound_ctrl:1\n"
      "v_cvt_pk_bf16_f32 v16, v16, v17\n"
      "v_min_u32 v16, -2, v16\n"
      "s_mov_b64 exec, s[86:87]\n"
      "buffer_store_dword v16, v62, s[4:7], s62 offen sc1\n"
      "s_mov_b64 exec, -1\n"
      ".Lfd_rf_28:\n"
      "s_waitcnt lgkmcnt(0)\n"
      "s_barrier\n"
      ".Lfd_rend_25:\n"
      "v_and_b32 v60, 7, v2\n"
      "v_lshlrev_b32 v60, 5, v60\n"
      "v_lshl_add_u32 v60, v3, 8, v60\n"
      "s_lshl_b32 s52, s40, 9\n"
      "v_add_u32 v60, s52, v60\n"
      "v_lshlrev_b32 v61, 2, v1\n"
      "v_lshlrev_b32 v62, 1, v1\n"
      "s_lshl_b32 s62, s44, 10\n"
      "s_lshl_b32 s53, s42, 7\n"
      "s_add_u32 s62, s62, s53\n"
      "s_add_u32 s62, s62, 1388544\n"
      "v_lshrrev_b32 v63, 5, v1\n"
      "v_lshlrev_b32 v63, 2, v63\n"
      "s_lshl_b32 s53, s40, 3\n"
      "v_add_u32 v63, s53, v63\n"
      "s_lshl_b32 s57, s43, 10\n"
      "s_add_u32 s57, s57, 1384448\n"
      "s_cmp_eq_u32 s47, 1\n"
      "s_cbranch_scc1 .Lfd_e_29\n"
      "s_sleep 1\n"
      "s_sleep 1\n"
      "s_sleep 1\n"
      "s_sleep 1\n"
      "s_sleep 1\n"
      "s_sleep 1\n"
      "s_sleep 1\n"
      "s_sleep 1\n"
      "s_sleep 1\n"
      "s_sleep 1\n"
      "s_sleep 1\n"
      "s_sleep 1\n"
      "s_sleep 1\n"
      "s_sleep 1\n"
      "s_sleep 1\n"
      "s_sleep 1\n"
      "s_branch .Lfd_e_30\n"
      ".Lfd_e_29:\n"
      "s_nop 5\n"
      "s_nop 5\n"
      "s_nop 5\n"
      "s_nop 5\n"
      "s_nop 5\n"
      "s_nop 5\n"
      "s_nop 5\n"
      "s_nop 5\n"
      "s_nop 5\n"
      "s_nop 5\n"
      "s_nop 5\n"
      "s_nop 5\n"
      ".Lfd_e_30:\n"
      "s_mov_b32 s56, 131072\n"
      ".Lfd_poll_31:\n"
      "buffer_load_dwordx4 v[16:19], v10, s[4:7], s57 offen sc1\n"
      "s_waitcnt vmcnt(0)\n"
      "v_max3_u32 v56, v16, v17, v18\n"
      "v_max_u32 v56, v56, v19\n"
      "v_cmp_eq_u32 vcc, -1, v56\n"
      "s_cbranch_vccz .Lfd_done_32\n"
      "s_sub_u32 s56, s56, 1\n"
      "s_cbranch_scc1 .Lfd_done_32\n"
      "s_sleep 1\n"
      "s_branch .Lfd_poll_31\n"
      ".Lfd_done_32:\n"
      "v_mfma_f32_16x16x32_bf16 v[24:27], v[16:19], v[128:131], 0\n"
      "v_mov_b32_dpp v20, v16 row_ror:8 row_mask:0xf bank_mask:0xf bound_ctrl:1\n"
      "v_mov_b32_dpp v21, v17 row_ror:8 row_mask:0xf bank_mask:0xf bound_ctrl:1\n"
      "v_mfma_f32_16x16x32_bf16 v[28:31], v[16:19], v[132:135], 0\n"
      "v_mov_b32_dpp v22, v18 row_ror:8 row_mask:0xf bank_mask:0xf bound_ctrl:1\n"
      "v_mov_b32_dpp v23, v19 row_ror:8 row_mask:0xf bank_mask:0xf bound_ctrl:1\n"
      "v_mfma_f32_16x16x32_bf16 v[32:35], v[16:19], v[136:139], 0\n"
      "v_lshlrev_b32 v58, 16, v16\n"
      "v_and_b32 v59, 0xffff0000, v16\n"
      "v_mfma_f32_16x16x32_bf16 v[36:39], v[16:19], v[140:143], 0\n"
      "v_add_f32 v56, v58, v59\n"
      "v_mul_f32 v57, v58, v58\n"
      "v_mfma_f32_16x16x32_bf16 v[40:43], v[16:19], v[144:147], 0\n"
      "v_fmac_f32 v57, v59, v59\n"
      "v_lshlrev_b32 v58, 16, v17\n"
      "v_mfma_f32_16x16x32_bf16 v[44:47], v[16:19], v[148:151], 0\n"
      "v_and_b32 v59, 0xffff0000, v17\n"
      "v_add_f32 v56, v56, v58\n"
      "v_mfma_f32_16x16x32_bf16 v[48:51], v[16:19], v[152:155], 0\n"
      "v_fmac_f32 v57, v58, v58\n"
      "v_add_f32 v56, v56, v59\n"
      "v_mfma_f32_16x16x32_bf16 v[52:55], v[16:19], v[156:159], 0\n"
      "v_fmac_f32 v57, v59, v59\n"
      "v_lshlrev_b32 v58, 16, v18\n"
      "v_mfma_f32_16x16x32_bf16 v[24:27], v[20:23], v[160:163], v[24:27]\n"
      "v_and_b32 v59, 0xffff0000, v18\n"
      "v_add_f32 v56, v56, v58\n"
      "v_mfma_f32_16x16x32_bf16 v[28:31], v[20:23], v[164:167], v[28:31]\n"
      "v_fmac_f32 v57, v58, v58\n"
      "v_add_f32 v56, v56, v59\n"
      "v_mfma_f32_16x16x32_bf16 v[32:35], v[20:23], v[168:171], v[32:35]\n"
      "v_fmac_f32 v57, v59, v59\n"
      "v_lshlrev_b32 v58, 16, v19\n"
      "v_mfma_f32_16x16x32_bf16 v[36:39], v[20:23], v[172:175], v[36:39]\n"
      "v_and_b32 v59, 0xffff0000, v19\n"
      "v_add_f32 v56, v56, v58\n"
      "v_mfma_f32_16x16x32_bf16 v[40:43], v[20:23], v[176:179], v[40:43]\n"
      "v_fmac_f32 v57, v58, v58\n"
      "v_add_f32 v56, v56, v59\n"
      "v_mfma_f32_16x16x32_bf16 v[44:47], v[20:23], v[180:183], v[44:47]\n"
      "v_fmac_f32 v57, v59, v59\n"
      "v_add_f32_dpp v56, v56, v56 row_ror:4 row_mask:0xf bank_mask:0xf bound_ctrl:1\n"
      "v_mfma_f32_16x16x32_bf16 v[48:51], v[20:23], v[184:187], v[48:51]\n"
      "v_add_f32_dpp v57, v57, v57 row_ror:4 row_mask:0xf bank_mask:0xf bound_ctrl:1\n"
      "v_add_f32_dpp v56, v56, v56 row_ror:8 row_mask:0xf bank_mask:0xf bound_ctrl:1\n"
      "v_mfma_f32_16x16x32_bf16 v[52:55], v[20:23], v[188:191], v[52:55]\n"
      "v_add_f32_dpp v57, v57, v57 row_ror:8 row_mask:0xf bank_mask:0xf bound_ctrl:1\n"
      "s_nop 1\n"
      "v_permlane32_swap_b32 v56, v57\n"
      "v_add_f32 v56, v56, v57\n"
      "v_mov_b32 v57, v56\n"
      "s_nop 1\n"
      "v_permlane16_swap_b32 v56, v57\n"
      "v_add_f32 v56, v56, v57\n"
      "s_mov_b64 exec, s[92:93]\n"
      "ds_write_b32 v63, v56 offset:4096\n"
      "s_mov_b64 exec, s[90:91]\n"
      "ds_write_b128 v10, v[16:19] offset:5632\n"
      "s_mov_b64 exec, s[88:89]\n"
      "ds_write2_b32 v60, v24, v28 offset0:0 offset1:1\n"
      "ds_write2_b32 v60, v32, v36 offset0:2 offset1:3\n"
      "ds_write2_b32 v60, v40, v44 offset0:4 offset1:5\n"
      "ds_write2_b32 v60, v48, v52 offset0:6 offset1:7\n"
      "s_mov_b64 exec, -1\n"
      "s_waitcnt lgkmcnt(0)\n"
      "s_barrier\n"
      "s_cmp_lg_u32 s40, 0\n"
      "s_cbranch_scc1 .Lfd_qf_33\n"
      "ds_read2st64_b32 v[16:17], v61 offset0:0 offset1:1\n"
      "ds_read2st64_b32 v[18:19], v61 offset0:2 offset1:3\n"
      "ds_read2st64_b32 v[20:21], v61 offset0:4 offset1:5\n"
      "ds_read2st64_b32 v[22:23], v61 offset0:6 offset1:7\n"
      "v_mov_b32 v32, 0\n"
      "ds_read_b128 v[24:27], v32 offset:4096\n"
      "ds_read_b128 v[28:31], v32 offset:4112\n"
      "s_waitcnt lgkmcnt(2)\n"
      "v_pk_add_f32 v[16:17], v[16:17], v[18:19]\n"
      "v_pk_add_f32 v[20:21], v[20:21], v[22:23]\n"
      "v_pk_add_f32 v[16:17], v[16:17], v[20:21]\n"
      "v_add_f32 v16, v16, v17\n"
      "s_waitcnt lgkmcnt(0)\n"
      "v_pk_add_f32 v[24:25], v[24:25], v[26:27]\n"
      "v_pk_add_f32 v[28:29], v[28:29], v[30:31]\n"
      "v_pk_add_f32 v[24:25], v[24:25], v[28:29]\n"
      "v_pk_mul_f32 v[24:25], v[24:25], s[98:99]\n"
      "v_fma_f32 v25, -v24, v24, v25\n"
      "v_fma_f32 v16, -v24, v4, v16\n"
      "v_max_f32 v25, 0, v25\n"
      "v_add_f32 v25, 0x3727c5ac, v25\n"
      "v_rsq_f32 v25, v25\n"
      "s_nop 0\n"
      "v_mul_f32 v16, v16, v25\n"
      "s_nop 1\n"
      "v_mov_b32_dpp v17, v16 quad_perm:[1,0,3,2] row_mask:0xf bank_mask:0xf bound_ctrl:1\n"
      "v_cvt_pk_bf16_f32 v16, v16, v17\n"
      "v_min_u32 v16, -2, v16\n"
      "s_mov_b64 exec, s[86:87]\n"
      "buffer_store_dword v16, v62, s[4:7], s62 offen sc1\n"
      "s_mov_b64 exec, -1\n"
      ".Lfd_qf_33:\n"
      "v_lshrrev_b32 v40, 2, v2\n"
      "v_lshl_add_u32 v40, v40, 2, v3\n"
      "v_lshlrev_b32 v40, 4, v40\n"
      "v_and_b32 v41, 3, v2\n"
      "v_lshl_add_u32 v40, v41, 8, v40\n"
      "s_lshl_b32 s57, s44, 10\n"
      "s_add_u32 s57, s57, 1388544\n"
      "v_mul_u32_u24 v54, 528, v2\n"
      "v_lshl_add_u32 v54, v3, 4, v54\n"
      "s_mul_i32 s52, s40, 2112\n"
      "s_add_u32 s52, s52, 9472\n"
      "v_add_u32 v54, s52, v54\n"
      "v_lshlrev_b32 v55, 4, v1\n"
      "v_lshl_add_u32 v55, v3, 4, v55\n"
      "v_add_u32 v55, 7680, v55\n"
      "v_cmp_gt_u32 vcc, 4, v2\n"
      "s_nop 1\n"
      "v_cndmask_b32 v54, v55, v54, vcc\n"
      "v_lshlrev_b32 v55, 3, v1\n"
      "s_lshl_b32 s52, s40, 5\n"
      "s_add_u32 s52, s52, 4160\n"
      "v_add_u32 v60, s52, v55\n"
      "v_add_u32 v55, 7680, v55\n"
      "v_cmp_gt_u32 vcc, 4, v1\n"
      "s_nop 1\n"
      "v_cndmask_b32 v55, v55, v60, vcc\n"
      "v_lshlrev_b32 v60, 3, v1\n"
      "s_mul_i32 s52, s40, 528\n"
      "v_add_u32 v60, s52, v60\n"
      "s_lshl_b32 s52, s40, 3\n"
      "v_mov_b32 v61, s52\n"
      "v_lshlrev_b32 v62, 2, v1\n"
      "s_lshl_b32 s52, s44, 3\n"
      "s_add_u32 s52, s52, s42\n"
      "s_lshl_b32 s52, s52, 2\n"
      "s_add_u32 s52, s52, s40\n"
      "s_mul_i32 s62, s52, 384\n"
      "s_add_u32 s62, s62, 466944\n"
      "s_nop 5\n"
      "s_nop 5\n"
      "s_nop 5\n"
      "s_nop 5\n"
      "s_nop 5\n"
      "s_nop 5\n"
      "s_nop 5\n"
      "s_nop 5\n"
      "s_nop 5\n"
      "s_nop 5\n"
      "s_nop 5\n"
      "s_nop 5\n"
      "s_cmp_eq_u32 s40, 0\n"
      "s_cbranch_scc1 .Lfd_wd_34\n"
      "s_nop 5\n"
      "s_nop 5\n"
      "s_nop 5\n"
      "s_nop 5\n"
      ".Lfd_wd_34:\n"
      "s_mov_b32 s56, 131072\n"
      ".Lfd_poll_35:\n"
      "buffer_load_dwordx4 v[16:19], v40, s[4:7], s57 offen sc1\n"
      "s_waitcnt vmcnt(0)\n"
      "v_max3_u32 v41, v16, v17, v18\n"
      "v_max_u32 v41, v41, v19\n"
      "v_cmp_eq_u32 vcc, -1, v41\n"
      "s_cbranch_vccz .Lfd_done_36\n"
      "s_sub_u32 s56, s56, 1\n"
      "s_cbranch_scc1 .Lfd_done_36\n"
      "s_sleep 1\n"
      "s_branch .Lfd_poll_35\n"
      ".Lfd_done_36:\n"
      "v_mfma_f32_16x16x32_bf16 v[32:35], a[0:3], v[16:19], 0\n"
      "v_mov_b32_dpp v20, v16 row_ror:12 row_mask:0xf bank_mask:0xf bound_ctrl:1\n"
      "v_mov_b32_dpp v21, v17 row_ror:12 row_mask:0xf bank_mask:0xf bound_ctrl:1\n"
      "v_mfma_f32_16x16x32_bf16 v[36:39], a[16:19], v[16:19], 0\n"
      "v_mov_b32_dpp v22, v18 row_ror:12 row_mask:0xf bank_mask:0xf bound_ctrl:1\n"
      "v_mov_b32_dpp v23, v19 row_ror:12 row_mask:0xf bank_mask:0xf bound_ctrl:1\n"
      "v_mov_b32_dpp v24, v16 row_ror:8 row_mask:0xf bank_mask:0xf bound_ctrl:1\n"
      "v_mov_b32_dpp v25, v17 row_ror:8 row_mask:0xf bank_mask:0xf bound_ctrl:1\n"
      "v_mfma_f32_16x16x32_bf16 v[32:35], a[4:7], v[20:23], v[32:35]\n"
      "v_mov_b32_dpp v26, v18 row_ror:8 row_mask:0xf bank_mask:0xf bound_ctrl:1\n"
      "v_mov_b32_dpp v27, v19 row_ror:8 row_mask:0xf bank_mask:0xf bound_ctrl:1\n"
      "v_mfma_f32_16x16x32_bf16 v[36:39], a[20:23], v[20:23], v[36:39]\n"
      "v_mov_b32_dpp v28, v16 row_ror:4 row_mask:0xf bank_mask:0xf bound_ctrl:1\n"
      "v_mov_b32_dpp v29, v17 row_ror:4 row_mask:0xf bank_mask:0xf bound_ctrl:1\n"
      "v_mfma_f32_16x16x32_bf16 v[32:35], a[8:11], v[24:27], v[32:35]\n"
      "v_mov_b32_dpp v30, v18 row_ror:4 row_mask:0xf bank_mask:0xf bound_ctrl:1\n"
      "v_mov_b32_dpp v31, v19 row_ror:4 row_mask:0xf bank_mask:0xf bound_ctrl:1\n"
      "v_mfma_f32_16x16x32_bf16 v[36:39], a[24:27], v[24:27], v[36:39]\n"
      "s_nop 0\n"
      "v_mfma_f32_16x16x32_bf16 v[32:35], a[12:15], v[28:31], v[32:35]\n"
      "v_mfma_f32_16x16x32_bf16 v[36:39], a[28:31], v[28:31], v[36:39]\n"
      "s_nop 6\n"
      "v_max3_f32 v40, v32, v33, v34\n"
      "v_max3_f32 v40, v40, v35, v36\n"
      "v_max3_f32 v40, v40, v37, v38\n"
      "v_max_f32 v40, v40, v39\n"
      "v_mov_b32 v41, v40\n"
      "s_nop 1\n"
      "v_permlane16_swap_b32 v40, v41\n"
      "v_max_f32 v40, v40, v41\n"
      "v_mov_b32 v41, v40\n"
      "s_nop 1\n"
      "v_permlane32_swap_b32 v40, v41\n"
      "v_max_f32 v40, v40, v41\n"
      "v_mul_f32 v52, s63, v40\n"
      "v_fma_f32 v32, v32, s63, -v52\n"
      "v_fma_f32 v33, v33, s63, -v52\n"
      "v_fma_f32 v34, v34, s63, -v52\n"
      "v_fma_f32 v35, v35, s63, -v52\n"
      "v_fma_f32 v36, v36, s63, -v52\n"
      "v_fma_f32 v37, v37, s63, -v52\n"
      "v_fma_f32 v38, v38, s63, -v52\n"
      "v_fma_f32 v39, v39, s63, -v52\n"
      "v_exp_f32 v32, v32\n"
      "v_exp_f32 v33, v33\n"
      "v_exp_f32 v34, v34\n"
      "v_exp_f32 v35, v35\n"
      "v_exp_f32 v36, v36\n"
      "v_exp_f32 v37, v37\n"
      "v_exp_f32 v38, v38\n"
      "v_exp_f32 v39, v39\n"
      "v_cvt_pk_bf16_f32 v48, v32, v33\n"
      "v_cvt_pk_bf16_f32 v49, v34, v35\n"
      "v_cvt_pk_bf16_f32 v50, v36, v37\n"
      "v_cvt_pk_bf16_f32 v51, v38, v39\n"
      "v_pk_add_f32 v[40:41], v[32:33], v[34:35]\n"
      "v_pk_add_f32 v[42:43], v[36:37], v[38:39]\n"
      "v_pk_add_f32 v[40:41], v[40:41], v[42:43]\n"
      "v_add_f32 v53, v40, v41\n"
      "v_mfma_f32_16x16x32_bf16 v[16:19], a[32:35], v[48:51], 0\n"
      "v_mov_b32 v58, v53\n"
      "s_nop 1\n"
      "v_permlane32_swap_b32 v53, v58\n"
      "v_mfma_f32_16x16x32_bf16 v[20:23], a[36:39], v[48:51], 0\n"
      "v_add_f32 v53, v53, v58\n"
      "v_mov_b32 v58, v53\n"
      "v_mfma_f32_16x16x32_bf16 v[24:27], a[40:43], v[48:51], 0\n"
      "s_nop 0\n"
      "v_permlane16_swap_b32 v53, v58\n"
      "v_add_f32 v53, v53, v58\n"
      "v_mfma_f32_16x16x32_bf16 v[28:31], a[44:47], v[48:51], 0\n"
      "ds_write_b64 v55, v[52:53]\n"
      "v_mfma_f32_16x16x32_bf16 v[32:35], a[48:51], v[48:51], 0\n"
      "ds_write_b128 v54, v[16:19] offset:0\n"
      "v_mfma_f32_16x16x32_bf16 v[36:39], a[52:55], v[48:51], 0\n"
      "ds_write_b128 v54, v[20:23] offset:64\n"
      "v_mfma_f32_16x16x32_bf16 v[40:43], a[56:59], v[48:51], 0\n"
      "ds_write_b128 v54, v[24:27] offset:128\n"
      "v_mfma_f32_16x16x32_bf16 v[44:47], a[60:63], v[48:51], 0\n"
      "ds_write_b128 v54, v[28:31] offset:192\n"
      "s_nop 0\n"
      "ds_write_b128 v54, v[32:35] offset:256\n"
      "s_nop 0\n"
      "ds_write_b128 v54, v[36:39] offset:320\n"
      "s_nop 0\n"
      "ds_write_b128 v54, v[40:43] offset:384\n"
      "s_nop 0\n"
      "ds_write_b128 v54, v[44:47] offset:448\n"
      "s_waitcnt lgkmcnt(0)\n"
      "s_barrier\n"
      "ds_read_b64 v[16:17], v61 offset:4160\n"
      "ds_read_b64 v[18:19], v61 offset:4192\n"
      "ds_read_b64 v[20:21], v61 offset:4224\n"
      "ds_read_b64 v[22:23], v61 offset:4256\n"
      "ds_read_b64 v[24:25], v60 offset:9472\n"
      "ds_read_b64 v[26:27], v60 offset:11584\n"
      "ds_read_b64 v[28:29], v60 offset:13696\n"
      "ds_read_b64 v[30:31], v60 offset:15808\n"
      "s_waitcnt lgkmcnt(4)\n"
      "v_max3_f32 v40, v16, v18, v20\n"
      "v_max_f32 v40, v40, v22\n"
      "v_sub_f32 v32, v16, v40\n"
      "v_sub_f32 v34, v18, v40\n"
      "v_sub_f32 v36, v20, v40\n"
      "v_sub_f32 v38, v22, v40\n"
      "v_exp_f32 v32, v32\n"
      "v_exp_f32 v34, v34\n"
      "v_exp_f32 v36, v36\n"
      "v_exp_f32 v38, v38\n"
      "s_waitcnt lgkmcnt(0)\n"
      "v_pk_mul_f32 v[42:43], v[32:33], v[24:25] op_sel_hi:[0,1]\n"
      "v_mul_f32 v41, v32, v17\n"
      "v_pk_fma_f32 v[42:43], v[34:35], v[26:27], v[42:43] op_sel_hi:[0,1,1]\n"
      "v_fmac_f32 v41, v34, v19\n"
      "v_pk_fma_f32 v[42:43], v[36:37], v[28:29], v[42:43] op_sel_hi:[0,1,1]\n"
      "v_fmac_f32 v41, v36, v21\n"
      "v_pk_fma_f32 v[42:43], v[38:39], v[30:31], v[42:43] op_sel_hi:[0,1,1]\n"
      "v_fmac_f32 v41, v38, v23\n"
      "v_cvt_pk_bf16_f32 v42, v42, v43\n"
      "v_min_u32 v42, -2, v42\n"
      "buffer_store_dword v42, v62, s[4:7], s62 offen sc1\n"
      "v_min_u32 v40, -2, v40\n"
      "v_min_u32 v41, -2, v41\n"
      "v_mov_b32 v44, 0\n"
      "s_mov_b64 exec, 1\n"
      "buffer_store_dwordx2 v[40:41], v44, s[4:7], s62 offen offset:256 sc1\n"
      "s_mov_b64 exec, -1\n"
      "v_and_b32 v56, 7, v2\n"
      "v_mul_u32_u24 v56, 1536, v56\n"
      "v_lshrrev_b32 v57, 3, v2\n"
      "v_lshl_add_u32 v56, v57, 6, v56\n"
      "v_lshl_add_u32 v56, v3, 4, v56\n"
      "v_and_b32 v57, 1, v3\n"
      "v_mul_u32_u24 v57, 6144, v57\n"
      "v_add_u32 v57, 256, v57\n"
      "s_mul_i32 s57, s44, 12288\n"
      "s_mul_i32 s52, s40, 384\n"
      "s_add_u32 s57, s57, s52\n"
      "s_add_u32 s57, s57, 466944\n"
      "s_add_u32 s59, s57, 3072\n"
      "v_lshlrev_b32 v11, 4, v2\n"
      "v_lshl_add_u32 v11, v3, 2, v11\n"
      "s_lshl_b32 s52, s40, 8\n"
      "v_add_u32 v11, s52, v11\n"
      "v_lshlrev_b32 v12, 2, v1\n"
      "v_lshlrev_b32 v13, 1, v1\n"
      "s_lshl_b32 s62, s44, 10\n"
      "s_lshl_b32 s53, s42, 7\n"
      "s_add_u32 s62, s62, s53\n"
      "s_add_u32 s62, s62, 892928\n"
      "s_nop 5\n"
      "s_nop 5\n"
      "s_nop 5\n"
      "s_nop 5\n"
      "s_nop 5\n"
      "s_nop 5\n"
      "s_nop 5\n"
      "s_nop 5\n"
      "s_nop 5\n"
      "s_nop 5\n"
      "s_nop 5\n"
      "s_nop 5\n"
      "s_cmp_eq_u32 s40, 0\n"
      "s_cbranch_scc1 .Lfd_wd_37\n"
      "s_nop 5\n"
      "s_nop 5\n"
      "s_nop 5\n"
      "s_nop 5\n"
      ".Lfd_wd_37:\n"
      "s_mov_b32 s56, 131072\n"
      ".Lfd_poll_38:\n"
      "buffer_load_dwordx2 v[24:25], v57, s[4:7], s57 offen sc1\n"
      "buffer_load_dwordx2 v[26:27], v57, s[4:7], s57 offen offset:1536 sc1\n"
      "buffer_load_dwordx2 v[28:29], v57, s[4:7], s59 offen sc1\n"
      "buffer_load_dwordx2 v[30:31], v57, s[4:7], s59 offen offset:1536 sc1\n"
      "buffer_load_dwordx4 v[16:19], v56, s[4:7], s57 offen sc1\n"
      "buffer_load_dwordx4 v[20:23], v56, s[4:7], s57 offen offset:128 sc1\n"
      "s_waitcnt vmcnt(4)\n"
      "v_max3_u32 v58, v24, v25, v26\n"
      "s_waitcnt vmcnt(3)\n"
      "v_max3_u32 v58, v58, v27, v28\n"
      "s_waitcnt vmcnt(2)\n"
      "v_max3_u32 v58, v58, v29, v30\n"
      "s_waitcnt vmcnt(1)\n"
      "v_max3_u32 v58, v58, v31, v16\n"
      "v_max3_u32 v58, v58, v17, v18\n"
      "s_waitcnt vmcnt(0)\n"
      "v_max3_u32 v58, v58, v19, v20\n"
      "v_max3_u32 v58, v58, v21, v22\n"
      "v_max_u32 v58, v58, v23\n"
      "v_cmp_eq_u32 vcc, -1, v58\n"
      "s_cbranch_vccz .Lfd_done_39\n"
      "s_sub_u32 s56, s56, 1\n"
      "s_cbranch_scc1 .Lfd_done_39\n"
      "s_sleep 1\n"
      "s_branch .Lfd_poll_38\n"
      ".Lfd_done_39:\n"
      "v_mfma_f32_16x16x32_bf16 v[40:43], v[16:19], v[192:195], 0\n"
      "v_mov_b32_dpp v32, v16 row_ror:8 row_mask:0xf bank_mask:0xf bound_ctrl:1\n"
      "v_mov_b32_dpp v33, v17 row_ror:8 row_mask:0xf bank_mask:0xf bound_ctrl:1\n"
      "v_mfma_f32_16x16x32_bf16 v[44:47], v[16:19], v[196:199], 0\n"
      "v_mov_b32_dpp v34, v18 row_ror:8 row_mask:0xf bank_mask:0xf bound_ctrl:1\n"
      "v_mov_b32_dpp v35, v19 row_ror:8 row_mask:0xf bank_mask:0xf bound_ctrl:1\n"
      "v_mfma_f32_16x16x32_bf16 v[48:51], v[16:19], v[200:203], 0\n"
      "v_mov_b32_dpp v36, v20 row_ror:8 row_mask:0xf bank_mask:0xf bound_ctrl:1\n"
      "v_mov_b32_dpp v37, v21 row_ror:8 row_mask:0xf bank_mask:0xf bound_ctrl:1\n"
      "v_mfma_f32_16x16x32_bf16 v[52:55], v[16:19], v[204:207], 0\n"
      "v_mov_b32_dpp v38, v22 row_ror:8 row_mask:0xf bank_mask:0xf bound_ctrl:1\n"
      "v_mov_b32_dpp v39, v23 row_ror:8 row_mask:0xf bank_mask:0xf bound_ctrl:1\n"
      "v_mfma_f32_16x16x32_bf16 v[40:43], v[20:23], v[224:227], v[40:43]\n"
      "v_max3_f32 v58, v24, v26, v28\n"
      "v_max_f32 v58, v58, v30\n"
      "v_mfma_f32_16x16x32_bf16 v[44:47], v[20:23], v[228:231], v[44:47]\n"
      "v_mov_b32 v59, v58\n"
      "s_nop 1\n"
      "v_permlane16_swap_b32 v58, v59\n"
      "v_mfma_f32_16x16x32_bf16 v[48:51], v[20:23], v[232:235], v[48:51]\n"
      "v_max_f32 v58, v58, v59\n"
      "v_sub_f32 v60, v24, v58\n"
      "v_mfma_f32_16x16x32_bf16 v[52:55], v[20:23], v[236:239], v[52:55]\n"
      "v_sub_f32 v61, v26, v58\n"
      "v_sub_f32 v62, v28, v58\n"
      "v_mfma_f32_16x16x32_bf16 v[40:43], v[32:35], v[208:211], v[40:43]\n"
      "v_sub_f32 v63, v30, v58\n"
      "v_exp_f32 v60, v60\n"
      "v_mfma_f32_16x16x32_bf16 v[44:47], v[32:35], v[212:215], v[44:47]\n"
      "v_exp_f32 v61, v61\n"
      "v_exp_f32 v62, v62\n"
      "v_mfma_f32_16x16x32_bf16 v[48:51], v[32:35], v[216:219], v[48:51]\n"
      "v_exp_f32 v63, v63\n"
      "v_mul_f32 v58, v60, v25\n"
      "v_mfma_f32_16x16x32_bf16 v[52:55], v[32:35], v[220:223], v[52:55]\n"
      "v_fmac_f32 v58, v61, v27\n"
      "v_fmac_f32 v58, v62, v29\n"
      "v_mfma_f32_16x16x32_bf16 v[40:43], v[36:39], v[240:243], v[40:43]\n"
      "v_fmac_f32 v58, v63, v31\n"
      "v_mov_b32 v59, v58\n"
      "v_mfma_f32_16x16x32_bf16 v[44:47], v[36:39], v[244:247], v[44:47]\n"
      "s_nop 0\n"
      "v_permlane16_swap_b32 v58, v59\n"
      "v_add_f32 v58, v58, v59\n"
      "v_mfma_f32_16x16x32_bf16 v[48:51], v[36:39], v[248:251], v[48:51]\n"
      "v_rcp_f32 v58, v58\n"
      "v_mfma_f32_16x16x32_bf16 v[52:55], v[36:39], v[252:255], v[52:55]\n"
      "v_pk_mul_f32 v[60:61], v[60:61], v[58:59] op_sel_hi:[1,0]\n"
      "v_pk_mul_f32 v[62:63], v[62:63], v[58:59] op_sel_hi:[1,0]\n"
      "v_pk_mul_f32 v[24:25], v[60:61], v[40:41]\n"
      "v_pk_fma_f32 v[24:25], v[62:63], v[42:43], v[24:25]\n"
      "v_pk_mul_f32 v[26:27], v[60:61], v[44:45]\n"
      "v_pk_fma_f32 v[26:27], v[62:63], v[46:47], v[26:27]\n"
      "v_pk_mul_f32 v[28:29], v[60:61], v[48:49]\n"
      "v_pk_fma_f32 v[28:29], v[62:63], v[50:51], v[28:29]\n"
      "v_pk_mul_f32 v[30:31], v[60:61], v[52:53]\n"
      "v_pk_fma_f32 v[30:31], v[62:63], v[54:55], v[30:31]\n"
      "v_add_f32 v16, v24, v25\n"
      "v_add_f32 v17, v26, v27\n"
      "v_add_f32 v18, v28, v29\n"
      "v_add_f32 v19, v30, v31\n"
      "v_permlane16_swap_b32 v16, v17\n"
      "s_nop 0\n"
      "v_permlane16_swap_b32 v18, v19\n"
      "v_add_f32 v16, v16, v17\n"
      "v_add_f32 v18, v18, v19\n"
      "s_mov_b64 exec, s[94:95]\n"
      "ds_write2_b32 v11, v16, v18 offset0:0 offset1:2\n"
      "s_mov_b64 exec, -1\n"
      "s_waitcnt lgkmcnt(0)\n"
      "s_barrier\n"
      "s_cmp_lg_u32 s40, 0\n"
      "s_cbranch_scc1 .Lfd_of_40\n"
      "ds_read2st64_b32 v[16:17], v12 offset0:0 offset1:1\n"
      "ds_read2st64_b32 v[18:19], v12 offset0:2 offset1:3\n"
      "s_waitcnt lgkmcnt(0)\n"
      "v_pk_add_f32 v[16:17], v[16:17], v[18:19]\n"
      "v_add_f32 v16, v16, v17\n"
      "s_nop 1\n"
      "v_mov_b32_dpp v17, v16 quad_perm:[1,0,3,2] row_mask:0xf bank_mask:0xf bound_ctrl:1\n"
      "v_cvt_pk_bf16_f32 v16, v16, v17\n"
      "v_min_u32 v16, -2, v16\n"
      "s_mov_b64 exec, s[86:87]\n"
      "buffer_store_dword v16, v13, s[4:7], s62 offen sc1\n"
      "s_mov_b64 exec, -1\n"
      ".Lfd_of_40:\n"
      "v_lshlrev_b32 v56, 10, v3\n"
      "v_lshl_add_u32 v56, v2, 4, v56\n"
      "s_lshl_b32 s57, s43, 13\n"
      "s_lshl_b32 s52, s40, 8\n"
      "s_add_u32 s57, s57, s52\n"
      "s_add_u32 s57, s57, 892928\n"
      "s_add_u32 s59, s57, 4096\n"
      "s_lshl_b32 s52, s40, 8\n"
      "v_and_b32 v57, 1, v3\n"
      "v_lshlrev_b32 v57, 3, v57\n"
      "v_lshl_add_u32 v57, v2, 4, v57\n"
      "v_add_u32 v57, s52, v57\n"
      "ds_read_b64 v[28:29], v57 offset:5632\n"
      "v_lshrrev_b32 v11, 5, v1\n"
      "v_lshlrev_b32 v11, 2, v11\n"
      "s_lshl_b32 s53, s40, 3\n"
      "v_add_u32 v11, s53, v11\n"
      "v_and_b32 v12, 1, v2\n"
      "v_lshlrev_b32 v12, 6, v12\n"
      "v_lshl_add_u32 v12, v3, 4, v12\n"
      "v_lshlrev_b32 v13, 1, v1\n"
      "s_lshl_b32 s53, s40, 4\n"
      "v_add_u32 v13, s53, v13\n"
      "v_lshlrev_b32 v14, 4, v3\n"
      "v_lshrrev_b32 v15, 3, v2\n"
      "v_mul_u32_u24 v15, 7168, v15\n"
      "v_and_b32 v58, 7, v2\n"
      "v_lshl_add_u32 v15, v58, 4, v15\n"
      "s_lshl_b32 s52, s43, 3\n"
      "s_lshl_b32 s53, s40, 1\n"
      "s_add_u32 s52, s52, s53\n"
      "s_mul_i32 s52, s52, 56\n"
      "s_add_u32 s52, s52, s45\n"
      "s_lshl_b32 s62, s52, 7\n"
      "s_add_u32 s62, s62, 1155072\n"
      "s_nop 5\n"
      "s_nop 5\n"
      "s_nop 5\n"
      "s_nop 5\n"
      "s_nop 5\n"
      "s_nop 5\n"
      "s_nop 5\n"
      "s_nop 5\n"
      "s_nop 5\n"
      "s_nop 5\n"
      "s_nop 5\n"
      "s_nop 5\n"
      "s_cmp_eq_u32 s40, 0\n"
      "s_cbranch_scc1 .Lfd_wd_41\n"
      "s_nop 5\n"
      "s_nop 5\n"
      "s_nop 5\n"
      "s_nop 5\n"
      ".Lfd_wd_41:\n"
      "s_mov_b32 s56, 131072\n"
      ".Lfd_poll_42:\n"
      "buffer_load_dwordx4 v[16:19], v56, s[4:7], s57 offen sc1\n"
      "buffer_load_dwordx4 v[20:23], v56, s[4:7], s59 offen sc1\n"
      "s_waitcnt vmcnt(1)\n"
      "v_max3_u32 v58, v16, v17, v18\n"
      "s_waitcnt vmcnt(0)\n"
      "v_max3_u32 v58, v58, v19, v20\n"
      "v_max3_u32 v58, v58, v21, v22\n"
      "v_max_u32 v58, v58, v23\n"
      "v_cmp_eq_u32 vcc, -1, v58\n"
      "s_cbranch_vccz .Lfd_done_43\n"
      "s_sub_u32 s56, s56, 1\n"
      "s_cbranch_scc1 .Lfd_done_43\n"
      "s_sleep 1\n"
      "s_branch .Lfd_poll_42\n"
      ".Lfd_done_43:\n"
      "v_mfma_f32_16x16x32_bf16 v[24:27], v[6:9], v[16:19], 0\n"
      "v_mfma_f32_16x16x32_bf16 v[24:27], v[6:9], v[20:23], v[24:27]\n"
      "s_waitcnt lgkmcnt(0)\n"
      "v_lshlrev_b32 v32, 16, v28\n"
      "v_and_b32 v33, 0xffff0000, v28\n"
      "v_lshlrev_b32 v34, 16, v29\n"
      "v_and_b32 v35, 0xffff0000, v29\n"
      "s_nop 2\n"
      "v_cvt_pk_bf16_f32 v30, v24, v25\n"
      "v_cvt_pk_bf16_f32 v31, v26, v27\n"
      "v_lshlrev_b32 v36, 16, v30\n"
      "v_and_b32 v37, 0xffff0000, v30\n"
      "v_lshlrev_b32 v38, 16, v31\n"
      "v_and_b32 v39, 0xffff0000, v31\n"
      "v_pk_add_f32 v[32:33], v[32:33], v[36:37]\n"
      "v_pk_add_f32 v[34:35], v[34:35], v[38:39]\n"
      "v_cvt_pk_bf16_f32 v30, v32, v33\n"
      "v_cvt_pk_bf16_f32 v31, v34, v35\n"
      "ds_write_b64 v57, v[30:31] offset:4608\n"
      "v_lshlrev_b32 v32, 16, v30\n"
      "v_and_b32 v33, 0xffff0000, v30\n"
      "v_lshlrev_b32 v34, 16, v31\n"
      "v_and_b32 v35, 0xffff0000, v31\n"
      "v_pk_add_f32 v[36:37], v[32:33], v[34:35]\n"
      "v_pk_mul_f32 v[38:39], v[32:33], v[32:33]\n"
      "v_pk_fma_f32 v[38:39], v[34:35], v[34:35], v[38:39]\n"
      "v_add_f32 v36, v36, v37\n"
      "v_add_f32 v37, v38, v39\n"
      "s_nop 1\n"
      "v_permlane32_swap_b32 v36, v37\n"
      "v_add_f32 v36, v36, v37\n"
      "v_mov_b32 v37, v36\n"
      "s_nop 1\n"
      "v_permlane16_swap_b32 v36, v37\n"
      "v_add_f32 v36, v36, v37\n"
      "s_nop 1\n"
      "v_add_f32_dpp v36, v36, v36 quad_perm:[1,0,3,2] row_mask:0xf bank_mask:0xf bound_ctrl:1\n"
      "s_nop 1\n"
      "v_add_f32_dpp v36, v36, v36 quad_perm:[2,3,0,1] row_mask:0xf bank_mask:0xf bound_ctrl:1\n"
      "s_nop 1\n"
      "v_add_f32_dpp v36, v36, v36 row_ror:4 row_mask:0xf bank_mask:0xf bound_ctrl:1\n"
      "s_nop 1\n"
      "v_add_f32_dpp v36, v36, v36 row_ror:8 row_mask:0xf bank_mask:0xf bound_ctrl:1\n"
      "s_mov_b64 exec, s[92:93]\n"
      "ds_write_b32 v11, v36 offset:4096\n"
      "s_mov_b64 exec, -1\n"
      "v_mov_b32 v59, 0\n"
      "s_waitcnt lgkmcnt(0)\n"
      "s_barrier\n"
      "s_cmp_ge_u32 s45, 56\n"
      "s_cbranch_scc1 .Lfd_f1_44\n"
      "ds_read_b128 v[16:19], v12 offset:4608\n"
      "ds_read_b128 v[20:23], v12 offset:4736\n"
      "ds_read_b128 v[24:27], v12 offset:4864\n"
      "ds_read_b128 v[28:31], v12 offset:4992\n"
      "ds_read_b128 v[32:35], v12 offset:5120\n"
      "ds_read_b128 v[36:39], v12 offset:5248\n"
      "ds_read_b128 v[40:43], v12 offset:5376\n"
      "ds_read_b128 v[44:47], v12 offset:5504\n"
      "ds_read_b128 v[48:51], v59 offset:4096\n"
      "ds_read_b128 v[52:55], v59 offset:4112\n"
      "s_waitcnt lgkmcnt(9)\n"
      "v_mfma_f32_16x16x32_bf16 v[60:63], v[16:19], a[64:67], 0\n"
      "s_waitcnt lgkmcnt(8)\n"
      "v_mfma_f32_16x16x32_bf16 v[60:63], v[20:23], a[68:71], v[60:63]\n"
      "s_waitcnt lgkmcnt(7)\n"
      "v_mfma_f32_16x16x32_bf16 v[60:63], v[24:27], a[72:75], v[60:63]\n"
      "s_waitcnt lgkmcnt(6)\n"
      "v_mfma_f32_16x16x32_bf16 v[60:63], v[28:31], a[76:79], v[60:63]\n"
      "s_waitcnt lgkmcnt(5)\n"
      "v_mfma_f32_16x16x32_bf16 v[60:63], v[32:35], a[80:83], v[60:63]\n"
      "s_waitcnt lgkmcnt(4)\n"
      "v_mfma_f32_16x16x32_bf16 v[60:63], v[36:39], a[84:87], v[60:63]\n"
      "s_waitcnt lgkmcnt(0)\n"
      "v_pk_add_f32 v[48:49], v[48:49], v[50:51]\n"
      "v_pk_add_f32 v[52:53], v[52:53], v[54:55]\n"
      "v_pk_add_f32 v[48:49], v[48:49], v[52:53]\n"
      "s_waitcnt lgkmcnt(3)\n"
      "v_mfma_f32_16x16x32_bf16 v[60:63], v[40:43], a[88:91], v[60:63]\n"
      "v_mul_f32 v48, 0x3a800000, v48\n"
      "v_mul_f32 v49, 0x3a800000, v49\n"
      "v_fma_f32 v49, -v48, v48, v49\n"
      "s_waitcnt lgkmcnt(2)\n"
      "v_mfma_f32_16x16x32_bf16 v[60:63], v[44:47], a[92:95], v[60:63]\n"
      "v_max_f32 v49, 0, v49\n"
      "v_add_f32 v49, 0x3727c5ac, v49\n"
      "v_rsq_f32 v49, v49\n"
      "s_nop 4\n"
      "v_cndmask_b32 v16, v61, v60, s[96:97]\n"
      "s_nop 1\n"
      "v_add_f32_dpp v16, v16, v16 row_ror:8 row_mask:0xf bank_mask:0xf bound_ctrl:1\n"
      "v_fma_f32 v16, -v48, v5, v16\n"
      "v_mul_f32 v16, v16, v49\n"
      "v_cvt_pk_bf16_f32 v16, v16, v16\n"
      "v_lshlrev_b32 v16, 16, v16\n"
      "v_mul_f32 v17, 0xbfb8aa3b, v16\n"
      "v_exp_f32 v17, v17\n"
      "s_nop 0\n"
      "v_add_f32 v17, 1.0, v17\n"
      "v_rcp_f32 v17, v17\n"
      "s_nop 0\n"
      "v_mul_f32 v16, v16, v17\n"
      "s_nop 1\n"
      "v_mov_b32_dpp v17, v16 quad_perm:[1,0,3,2] row_mask:0xf bank_mask:0xf bound_ctrl:1\n"
      "v_cvt_pk_bf16_f32 v16, v16, v17\n"
      "s_mov_b64 exec, 0x55\n"
      "ds_write_b32 v13, v16 offset:4288\n"
      "s_mov_b64 exec, -1\n"
      ".Lfd_f1_44:\n"
      "s_waitcnt lgkmcnt(0)\n"
      "s_barrier\n"
      "s_cmp_ge_u32 s45, 56\n"
      "s_cbranch_scc1 .Lfd_f2_45\n"
      "ds_read_b128 v[16:19], v14 offset:4288\n"
      "s_waitcnt lgkmcnt(0)\n"
      "v_mfma_f32_16x16x32_bf16 v[20:23], v[16:19], v[96:99], 0\n"
      "v_mfma_f32_16x16x32_bf16 v[24:27], v[16:19], v[100:103], 0\n"
      "v_mfma_f32_16x16x32_bf16 v[28:31], v[16:19], v[104:107], 0\n"
      "v_mfma_f32_16x16x32_bf16 v[32:35], v[16:19], v[108:111], 0\n"
      "v_mfma_f32_16x16x32_bf16 v[36:39], v[16:19], v[112:115], 0\n"
      "v_mfma_f32_16x16x32_bf16 v[40:43], v[16:19], v[116:119], 0\n"
      "v_mfma_f32_16x16x32_bf16 v[44:47], v[16:19], v[120:123], 0\n"
      "v_mfma_f32_16x16x32_bf16 v[48:51], v[16:19], v[124:127], 0\n"
      "s_nop 1\n"
      "v_cvt_pk_bf16_f32 v52, v20, v24\n"
      "v_min_u32 v52, -2, v52\n"
      "v_cvt_pk_bf16_f32 v53, v28, v32\n"
      "v_min_u32 v53, -2, v53\n"
      "v_cvt_pk_bf16_f32 v54, v36, v40\n"
      "v_min_u32 v54, -2, v54\n"
      "v_cvt_pk_bf16_f32 v55, v44, v48\n"
      "v_min_u32 v55, -2, v55\n"
      "s_mov_b64 exec, 0xffff\n"
      "buffer_store_dwordx4 v[52:55], v15, s[4:7], s62 offen sc1\n"
      "s_mov_b64 exec, -1\n"
      ".Lfd_f2_45:\n"
      "s_cmp_eq_u32 s47, 1\n"
      "s_cbranch_scc0 .Lfd_rend_46\n"
      "v_lshrrev_b32 v56, 3, v2\n"
      "v_lshl_add_u32 v56, v56, 2, v3\n"
      "v_lshlrev_b32 v56, 7, v56\n"
      "v_and_b32 v57, 7, v2\n"
      "v_lshl_add_u32 v56, v57, 4, v56\n"
      "s_lshl_b32 s52, s43, 3\n"
      "s_add_u32 s52, s52, s42\n"
      "s_mul_i32 s52, s52, 56\n"
      "s_lshl_b32 s53, s40, 4\n"
      "s_add_u32 s52, s52, s53\n"
      "s_lshl_b32 s57, s52, 7\n"
      "s_add_u32 s57, s57, 1155072\n"
      "s_add_u32 s59, s57, 1024\n"
      "s_cmp_eq_u32 s40, 3\n"
      "s_cselect_b32 s59, 1449984, s59\n"
      "v_lshlrev_b32 v57, 5, v2\n"
      "v_lshl_add_u32 v57, v3, 4, v57\n"
      "s_lshl_b32 s52, s40, 8\n"
      "v_add_u32 v57, s52, v57\n"
      "v_lshlrev_b32 v61, 2, v1\n"
      "v_lshlrev_b32 v62, 1, v1\n"
      "s_lshl_b32 s53, s42, 7\n"
      "v_add_u32 v63, s53, v62\n"
      "s_lshl_b32 s62, s43, 10\n"
      "s_add_u32 s62, s62, s53\n"
      "ds_read_u16 v28, v63 offset:4608\n"
      "s_nop 5\n"
      "s_nop 5\n"
      "s_nop 5\n"
      "s_nop 5\n"
      "s_nop 5\n"
      "s_nop 5\n"
      "s_nop 5\n"
      "s_nop 5\n"
      "s_nop 5\n"
      "s_nop 5\n"
      "s_nop 5\n"
      "s_nop 5\n"
      "s_mov_b32 s56, 131072\n"
      ".Lfd_poll_47:\n"
      "buffer_load_dwordx4 v[16:19], v56, s[4:7], s57 offen sc1\n"
      "buffer_load_dwordx4 v[20:23], v56, s[4:7], s59 offen sc1\n"
      "s_waitcnt vmcnt(1)\n"
      "v_max3_u32 v58, v16, v17, v18\n"
      "s_waitcnt vmcnt(0)\n"
      "v_max3_u32 v58, v58, v19, v20\n"
      "v_max3_u32 v58, v58, v21, v22\n"
      "v_max_u32 v58, v58, v23\n"
      "v_cmp_eq_u32 vcc, -1, v58\n"
      "s_cbranch_vccz .Lfd_done_48\n"
      "s_sub_u32 s56, s56, 1\n"
      "s_cbranch_scc1 .Lfd_done_48\n"
      "s_sleep 1\n"
      "s_branch .Lfd_poll_47\n"
      ".Lfd_done_48:\n"
      "v_mfma_f32_16x16x32_bf16 v[24:27], v[6:9], v[16:19], 0\n"
      "v_mfma_f32_16x16x32_bf16 v[24:27], v[6:9], v[20:23], v[24:27]\n"
      "s_waitcnt lgkmcnt(0)\n"
      "v_lshlrev_b32 v28, 16, v28\n"
      "s_nop 5\n"
      "v_add_f32_dpp v24, v24, v24 row_ror:8 row_mask:0xf bank_mask:0xf bound_ctrl:1\n"
      "v_add_f32_dpp v25, v25, v25 row_ror:8 row_mask:0xf bank_mask:0xf bound_ctrl:1\n"
      "v_add_f32_dpp v26, v26, v26 row_ror:8 row_mask:0xf bank_mask:0xf bound_ctrl:1\n"
      "v_add_f32_dpp v27, v27, v27 row_ror:8 row_mask:0xf bank_mask:0xf bound_ctrl:1\n"
      "s_mov_b64 exec, 0x00ff00ff\n"
      "ds_write_b128 v57, v[24:27] offset:0\n"
      "s_mov_b64 exec, -1\n"
      "s_waitcnt lgkmcnt(0)\n"
      "s_barrier\n"
      "s_cmp_lg_u32 s40, 0\n"
      "s_cbranch_scc1 .Lfd_rf_49\n"
      "ds_read2st64_b32 v[16:17], v61 offset0:0 offset1:1\n"
      "ds_read2st64_b32 v[18:19], v61 offset0:2 offset1:3\n"
      "s_waitcnt lgkmcnt(0)\n"
      "v_pk_add_f32 v[16:17], v[16:17], v[18:19]\n"
      "v_add_f32 v16, v16, v17\n"
      "v_cvt_pk_bf16_f32 v16, v16, v16\n"
      "v_lshlrev_b32 v16, 16, v16\n"
      "v_add_f32 v16, v16, v28\n"
      "s_nop 1\n"
      "v_mov_b32_dpp v17, v16 quad_perm:[1,0,3,2] row_mask:0xf bank_mask:0xf bound_ctrl:1\n"
      "v_cvt_pk_bf16_f32 v16, v16, v17\n"
      "s_mov_b64 exec, s[86:87]\n"
      "buffer_store_dword v16, v62, s[36:39], s62 offen\n"
      "s_mov_b64 exec, -1\n"
      ".Lfd_rf_49:\n"
      ".Lfd_rend_46:\n"
      :
      : [kp] "s"(kp), [wg] "s"(wg), [tid] "v"(tid)
      : "memory", "vcc", "scc",
      "v1", "v2", "v3", "v4", "v5", "v6", "v7", "v8", "v9", "v10", "v11", "v12", "v13", "v14", "v15", "v16",
      "v17", "v18", "v19", "v20", "v21", "v22", "v23", "v24", "v25", "v26", "v27", "v28", "v29", "v30", "v31", "v32",
      "v33", "v34", "v35", "v36", "v37", "v38", "v39", "v40", "v41", "v42", "v43", "v44", "v45", "v46", "v47", "v48",
      "v49", "v50", "v51", "v52", "v53", "v54", "v55", "v56", "v57", "v58", "v59", "v60", "v61", "v62", "v63", "v64",
      "v65", "v66", "v67", "v68", "v69", "v70", "v71", "v72", "v73", "v74", "v75", "v76", "v77", "v78", "v79", "v80",
      "v81", "v82", "v83", "v84", "v85", "v86", "v87", "v88", "v89", "v90", "v91", "v92", "v93", "v94", "v95", "v96",
      "v97", "v98", "v99", "v100", "v101", "v102", "v103", "v104", "v105", "v106", "v107", "v108", "v109", "v110", "v111", "v112",
      "v113", "v114", "v115", "v116", "v117", "v118", "v119", "v120", "v121", "v122", "v123", "v124", "v125", "v126", "v127", "v128",
      "v129", "v130", "v131", "v132", "v133", "v134", "v135", "v136", "v137", "v138", "v139", "v140", "v141", "v142", "v143", "v144",
      "v145", "v146", "v147", "v148", "v149", "v150", "v151", "v152", "v153", "v154", "v155", "v156", "v157", "v158", "v159", "v160",
      "v161", "v162", "v163", "v164", "v165", "v166", "v167", "v168", "v169", "v170", "v171", "v172", "v173", "v174", "v175", "v176",
      "v177", "v178", "v179", "v180", "v181", "v182", "v183", "v184", "v185", "v186", "v187", "v188", "v189", "v190", "v191", "v192",
      "v193", "v194", "v195", "v196", "v197", "v198", "v199", "v200", "v201", "v202", "v203", "v204", "v205", "v206", "v207", "v208",
      "v209", "v210", "v211", "v212", "v213", "v214", "v215", "v216", "v217", "v218", "v219", "v220", "v221", "v222", "v223", "v224",
      "v225", "v226", "v227", "v228", "v229", "v230", "v231", "v232", "v233", "v234", "v235", "v236", "v237", "v238", "v239", "v240",
      "v241", "v242", "v243", "v244", "v245", "v246", "v247", "v248", "v249", "v250", "v251", "v252", "v253", "v254", "v255", "a0",
      "a1", "a2", "a3", "a4", "a5", "a6", "a7", "a8", "a9", "a10", "a11", "a12", "a13", "a14", "a15", "a16",
      "a17", "a18", "a19", "a20", "a21", "a22", "a23", "a24", "a25", "a26", "a27", "a28", "a29", "a30", "a31", "a32",
      "a33", "a34", "a35", "a36", "a37", "a38", "a39", "a40", "a41", "a42", "a43", "a44", "a45", "a46", "a47", "a48",
      "a49", "a50", "a51", "a52", "a53", "a54", "a55", "a56", "a57", "a58", "a59", "a60", "a61", "a62", "a63", "a64",
      "a65", "a66", "a67", "a68", "a69", "a70", "a71", "a72", "a73", "a74", "a75", "a76", "a77", "a78", "a79", "a80",
      "a81", "a82", "a83", "a84", "a85", "a86", "a87", "a88", "a89", "a90", "a91", "a92", "a93", "a94", "a95", "a96",
      "a97", "a98", "a99", "a100", "a101", "a102", "a103", "a104", "a105", "a106", "a107", "a108", "a109", "a110", "a111", "a112",
      "a113", "a114", "a115", "a116", "a117", "a118", "a119", "a120", "a121", "a122", "a123", "a124", "a125", "a126", "a127", "a128",
      "a129", "a130", "a131", "a132", "a133", "a134", "a135", "a136", "a137", "a138", "a139", "a140", "a141", "a142", "a143", "a144",
      "a145", "a146", "a147", "a148", "a149", "a150", "a151", "a152", "a153", "a154", "a155", "a156", "a157", "a158", "a159", "a160",
      "a161", "a162", "a163", "a164", "a165", "a166", "a167", "a168", "a169", "a170", "a171", "a172", "a173", "a174", "a175", "a176",
      "a177", "a178", "a179", "a180", "a181", "a182", "a183", "a184", "a185", "a186", "a187", "a188", "a189", "a190", "a191", "a192",
      "a193", "a194", "a195", "a196", "a197", "a198", "a199", "a200", "a201", "a202", "a203", "a204", "a205", "a206", "a207", "a208",
      "a209", "a210", "a211", "a212", "a213", "a214", "a215", "a216", "a217", "a218", "a219", "a220", "a221", "a222", "a223", "a224",
      "a225", "a226", "a227", "a228", "a229", "a230", "a231", "a232", "a233", "a234", "a235", "a236", "a237", "a238", "a239", "a240",
      "a241", "a242", "a243", "a244", "a245", "a246", "a247", "a248", "a249", "a250", "a251", "a252", "a253", "a254", "a255", "s3",
      "s4", "s5", "s6", "s7", "s8", "s9", "s10", "s11", "s12", "s13", "s14", "s15", "s16", "s17", "s18", "s19",
      "s20", "s21", "s22", "s23", "s24", "s25", "s26", "s27", "s28", "s29", "s30", "s31", "s36", "s37", "s38", "s39",
      "s40", "s41", "s42", "s43", "s44", "s45", "s46", "s47", "s48", "s49", "s50", "s51", "s52", "s53", "s54", "s55",
      "s56", "s57", "s58", "s59", "s60", "s61", "s62", "s63", "s64", "s65", "s66", "s67", "s68", "s69", "s70", "s71",
      "s72", "s73", "s74", "s75", "s76", "s77", "s78", "s79", "s80", "s81", "s82", "s83", "s84", "s85", "s86", "s87",
      "s88", "s89", "s90", "s91", "s92", "s93", "s94", "s95", "s96", "s97", "s98", "s99");
}

extern "C" void kernel_launch(void* const* d_in, const int* in_sizes, int n_in, void* d_out, int out_size, void* d_ws, size_t ws_size, hipStream_t stream) {
  Params p;
  p.x = (const u16*)d_in[0]; p.kc = (const u16*)d_in[1]; p.vc = (const u16*)d_in[2]; p.wq = (const u16*)d_in[3]; p.wo = (const u16*)d_in[4];
  p.w1 = (const u16*)d_in[6]; p.w2 = (const u16*)d_in[7]; p.out = (u16*)d_out; p.ws = (unsigned char*)d_ws;
  (void)hipFuncSetAttribute((const void*)fused_decode, hipFuncAttributeMaxDynamicSharedMemorySize, LDS_BYTES);
  hipLaunchKernelGGL(fused_decode, dim3(256), dim3(256), LDS_BYTES, stream, p);
}
```
